```python
import torch

def _softmax(x, axis=-1):
    return torch.softmax(x, dim=axis)
def _gelu(x):
    return torch.nn.functional.gelu(x, approximate="tanh")
def _silu(x):
    return torch.nn.functional.silu(x)
def _sigmoid(x):
    return torch.sigmoid(x)
def _layernorm(x, axis=-1, eps=1e-5):
    m = x.mean(dim=axis, keepdim=True)
    v = x.var(dim=axis, keepdim=True, unbiased=False)
    return (x - m) / torch.sqrt(v + eps)
def _scatter_add(tgt, idx, src, axis=0):
    return tgt.clone().index_add_(axis, idx, src)
def _segment_sum(src, seg, n):
    out = torch.zeros((n,) + src.shape[1:], dtype=src.dtype)
    return out.index_add_(0, seg, src)
def _pad(x, pw):
    flat = tuple(v for pair in reversed(pw) for v in pair)
    return torch.nn.functional.pad(x, flat)

kernel_name = 'gated_mix__ssm_conv_s4d__ssm_conv_s5__prompt'

def setup_inputs():

    _w = (lambda a: (lambda t: t.to(torch.bfloat16) if t.is_floating_point() else t)(torch.as_tensor(a)))
    def _A_setup():
        Bsz, L, D, N = 1, 2048, 576, 128
        return {
            "u": _w((lambda *s: torch.randn(*s, dtype=torch.bfloat16) * 0.05)(Bsz, L, D)),
            "log_A_real": _w((lambda *s: torch.randn(*s, dtype=torch.bfloat16) * 3.2)(D, N) * 0.5),
            "A_imag": _w((lambda *s: torch.randn(*s, dtype=torch.bfloat16) * 0.05)(D, N) * 3.14),
            "C_real": _w((lambda *s: torch.randn(*s, dtype=torch.bfloat16) * 0.1)(D, N) / N ** 0.5),
            "C_imag": _w((lambda *s: torch.randn(*s, dtype=torch.bfloat16) * 0.1)(D, N) / N ** 0.5),
            "log_dt": _w((lambda *s: torch.randn(*s, dtype=torch.bfloat16) * 0.05)(D) * 0.5 - 4.0),
            "D_skip": _w((lambda *s: torch.randn(*s, dtype=torch.bfloat16) * 0.05)(D)),
        }
    _A_in = _A_setup()

    def _B_setup():
        B, S, D = 1, 2048, 576
        P = D
        Lambda_re = -0.5 * (lambda _x: torch.exp(torch.as_tensor(_x)))((lambda *s: torch.randn(*s, dtype=torch.bfloat16) * 0.2)(P))
        Lambda_im = (lambda *s: torch.randn(*s, dtype=torch.bfloat16) * 0.4)(P) * 1.0
        B_re = (lambda *s: torch.randn(*s, dtype=torch.bfloat16) * 0.2)(P, D) * (D ** -0.5)
        B_im = (lambda *s: torch.randn(*s, dtype=torch.bfloat16) * 0.2)(P, D) * (D ** -0.5)
        C_re = (lambda *s: torch.randn(*s, dtype=torch.bfloat16) * 0.2)(D, P) * (P ** -0.5)
        C_im = (lambda *s: torch.randn(*s, dtype=torch.bfloat16) * 0.2)(D, P) * (P ** -0.5)
        log_dt = (lambda *s: torch.randn(*s, dtype=torch.bfloat16) * 0.2)(P) * 0.3 - 2.0
        Dskip = (lambda *s: torch.randn(*s, dtype=torch.bfloat16) * 0.05)(D)
        return {
            "x": _w((lambda *s: torch.randn(*s, dtype=torch.bfloat16) * 0.05)(B, S, D)),
            "Lambda_re": _w(Lambda_re),
            "Lambda_im": _w(Lambda_im),
            "B_re": _w(B_re),
            "B_im": _w(B_im),
            "C_re": _w(C_re),
            "C_im": _w(C_im),
            "log_dt": _w(log_dt),
            "Dskip": _w(Dskip),
        }
    _B_in = _B_setup()

    return {
        "A_u": _A_in["u"],
        "A_log_A_real": _A_in["log_A_real"],
        "A_A_imag": _A_in["A_imag"],
        "A_C_real": _A_in["C_real"],
        "A_C_imag": _A_in["C_imag"],
        "A_log_dt": _A_in["log_dt"],
        "A_D_skip": _A_in["D_skip"],
        "B_x": _B_in["x"],
        "B_Lambda_re": _B_in["Lambda_re"],
        "B_Lambda_im": _B_in["Lambda_im"],
        "B_B_re": _B_in["B_re"],
        "B_B_im": _B_in["B_im"],
        "B_C_re": _B_in["C_re"],
        "B_C_im": _B_in["C_im"],
        "B_log_dt": _B_in["log_dt"],
        "B_Dskip": _B_in["Dskip"],
        "gate_w": _w((lambda *s: torch.randn(*s, dtype=torch.bfloat16) * 0.05)(576) * 16.0),
    }

def reference(A_u, A_log_A_real, A_A_imag, A_C_real, A_C_imag, A_log_dt, A_D_skip, B_x, B_Lambda_re, B_Lambda_im, B_B_re, B_B_im, B_C_re, B_C_im, B_log_dt, B_Dskip, gate_w):
    def _A_ref(u, log_A_real, A_imag, C_real, C_imag, log_dt, D_skip):
        L = u.shape[1]
        dt = torch.exp(log_dt)
        A = -torch.exp(log_A_real) + 1j * A_imag
        C = (C_real + 1j * C_imag) * dt[:, None]
        pos = (lambda x, dt: x.to(dt))(torch.arange(L), log_dt.dtype)
        P = torch.exp((A * dt[:, None])[:, :, None] * pos[None, None, :])
        K = 2.0 * torch.einsum("dn,dnl->dl", C, P).real
        L2 = 2 * L
        u_t = (lambda x, *d: x.permute(*d))(u, 0, 2, 1)
        Kf = (lambda x, *a, **k: torch.fft.rfft(x.float(), *a, **k))(K, L2)
        Uf = (lambda x, *a, **k: torch.fft.rfft(x.float(), *a, **k))(u_t, L2)
        y = (lambda x, *a, **k: torch.fft.irfft(x, *a, **k).to(torch.bfloat16))(Uf * Kf[None, :, :], L2)[:, :, :L]
        y = (lambda x, *d: x.permute(*d))(y, 0, 2, 1)
        return y + u * D_skip[None, None, :]

    def _B_ref(x, Lambda_re, Lambda_im, B_re, B_im, C_re, C_im, log_dt, Dskip):
        Bsz, L, H = x.shape
        Lam = Lambda_re + 1j * Lambda_im
        Bmat = B_re + 1j * B_im
        Cmat = C_re + 1j * C_im
        dt = torch.exp(log_dt)
        Lam_bar = torch.exp(dt * Lam)
        B_bar = ((Lam_bar - 1.0) / Lam)[:, None] * Bmat
        Bu = torch.einsum("bld,pd->blp", x + 0j, B_bar)
        h = torch.zeros_like(Bu[:, 0, :])
        hs = []
        for t in range(L):
            h = Lam_bar[None, :] * h + Bu[:, t, :]
            hs.append(h)
        hseq = torch.stack(hs, 1)
        y = 2.0 * torch.einsum("blp,dp->bld", hseq, Cmat).real + x * Dskip[None, None, :]
        return y

    x = A_u
    g = _sigmoid((x * gate_w).sum(-1, keepdims=True))
    a = _A_ref(x, A_log_A_real, A_A_imag, A_C_real, A_C_imag, A_log_dt, A_D_skip)
    b = _B_ref(x, B_Lambda_re, B_Lambda_im, B_B_re, B_B_im, B_C_re, B_C_im, B_log_dt, B_Dskip)
    return g * a + (1 - g) * b
```

```cpp
#include <hip/hip_runtime.h>
#include <hip/hip_bf16.h>
#include <stdint.h>

#define L_ 2048
#define D_ 576
#define N_ 128
typedef unsigned short u16;
__device__ __forceinline__ float bf2f(u16 v){ return __uint_as_float(((unsigned)v)<<16); }
__device__ __forceinline__ u16 f2bf(float f){ __hip_bfloat16 b = __float2bfloat16(f); return *reinterpret_cast<u16*>(&b); }
__device__ __forceinline__ float rbf(float f){ return bf2f(f2bf(f)); }

__global__ void k_kgen(const u16* logA, const u16* Aim, const u16* Cre, const u16* Cim, const u16* logdt, float* K){
  int d = blockIdx.x;
  __shared__ float ar[N_], ai[N_], cr[N_], ci[N_];
  float dt = rbf(expf(bf2f(logdt[d])));
  if (threadIdx.x < N_){ int n = threadIdx.x;
    float are = -rbf(expf(bf2f(logA[d*N_+n])));
    ar[n] = are*dt; ai[n] = bf2f(Aim[d*N_+n])*dt; cr[n] = bf2f(Cre[d*N_+n])*dt; ci[n] = bf2f(Cim[d*N_+n])*dt; }
  __syncthreads();
  for (int l = threadIdx.x; l < L_; l += blockDim.x){
    float pos = rbf((float)l); float s = 0.f;
    for (int n = 0; n < N_; ++n){ float e = expf(ar[n]*pos); float sn, cs; sincosf(ai[n]*pos, &sn, &cs); s += e*(cr[n]*cs - ci[n]*sn); }
    K[d*L_+l] = 2.f*s;
  }
}
__global__ void k_conv(const u16* u, const float* K, const u16* Dsk, float* a){
  int d = blockIdx.x; __shared__ float sk[L_], su[L_];
  for (int l = threadIdx.x; l < L_; l += blockDim.x){ sk[l] = K[d*L_+l]; su[l] = bf2f(u[l*D_+d]); }
  __syncthreads();
  for (int t = threadIdx.x; t < L_; t += blockDim.x){ float s = 0.f; for (int l = 0; l <= t; ++l) s += sk[l]*su[t-l];
    a[t*D_+d] = rbf(rbf(s) + rbf(su[t]*bf2f(Dsk[d]))); }
}
__global__ void k_gemm1(const u16* x, const u16* Bre, const u16* Bim, const u16* Lre, const u16* Lim, const u16* logdt, float2* Bu){
  int t = blockIdx.x; __shared__ float sx[D_];
  for (int i = threadIdx.x; i < D_; i += blockDim.x) sx[i] = bf2f(x[t*D_+i]);
  __syncthreads();
  for (int p = threadIdx.x; p < D_; p += blockDim.x){
    float sr = 0, si = 0; for (int d = 0; d < D_; ++d){ sr += sx[d]*bf2f(Bre[p*D_+d]); si += sx[d]*bf2f(Bim[p*D_+d]); }
    float dt = rbf(expf(bf2f(logdt[p]))); float lr = bf2f(Lre[p]), li = bf2f(Lim[p]);
    float e = expf(dt*lr); float sn, cs; sincosf(dt*li, &sn, &cs); float br = e*cs, bi = e*sn;
    float nr = br-1.f, ni = bi; float den = lr*lr+li*li; float cr = (nr*lr+ni*li)/den, ci = (ni*lr-nr*li)/den;
    Bu[t*D_+p] = make_float2(cr*sr-ci*si, cr*si+ci*sr);
  }
}
__global__ void k_scan(const u16* Lre, const u16* Lim, const u16* logdt, float2* Bu){
  int p = blockIdx.x*blockDim.x+threadIdx.x; if (p >= D_) return;
  float dt = rbf(expf(bf2f(logdt[p]))); float lr = bf2f(Lre[p]), li = bf2f(Lim[p]);
  float e = expf(dt*lr); float sn, cs; sincosf(dt*li, &sn, &cs); float br = e*cs, bi = e*sn;
  float hr = 0, hi = 0;
  for (int t = 0; t < L_; ++t){ float2 b = Bu[t*D_+p]; float nr = br*hr-bi*hi+b.x, ni = br*hi+bi*hr+b.y; hr = nr; hi = ni; Bu[t*D_+p] = make_float2(hr,hi); }
}
__global__ void k_out(const u16* x, const u16* gw, const float2* h, const u16* Cre, const u16* Cim, const u16* Dsk, const float* a, float* out){
  int t = blockIdx.x; __shared__ float2 sh[D_]; __shared__ float red[256];
  float part = 0.f;
  for (int i = threadIdx.x; i < D_; i += blockDim.x){ sh[i] = h[t*D_+i]; part += rbf(bf2f(x[t*D_+i])*bf2f(gw[i])); }
  red[threadIdx.x] = part; __syncthreads();
  for (int s = 128; s > 0; s >>= 1){ if (threadIdx.x < s) red[threadIdx.x] += red[threadIdx.x+s]; __syncthreads(); }
  float logit = rbf(red[0]); float g = rbf(1.f/(1.f+expf(-logit))); float omg = rbf(1.f-g);
  for (int d = threadIdx.x; d < D_; d += blockDim.x){
    float s = 0; for (int p = 0; p < D_; ++p) s += sh[p].x*bf2f(Cre[d*D_+p]) - sh[p].y*bf2f(Cim[d*D_+p]);
    float b = 2.f*s + rbf(bf2f(x[t*D_+d])*bf2f(Dsk[d]));
    out[t*D_+d] = rbf(g*a[t*D_+d]) + omg*b;
  }
}
extern "C" void kernel_launch(void* const* d_in, const int* in_sizes, int n_in, void* d_out, int out_size, void* d_ws, size_t ws_size, hipStream_t stream){
  const u16* u=(const u16*)d_in[0]; const u16* logA=(const u16*)d_in[1]; const u16* Aim=(const u16*)d_in[2];
  const u16* Cre=(const u16*)d_in[3]; const u16* Cim=(const u16*)d_in[4]; const u16* logdt=(const u16*)d_in[5]; const u16* Dsk=(const u16*)d_in[6];
  const u16* Lre=(const u16*)d_in[8]; const u16* Lim=(const u16*)d_in[9]; const u16* Bre=(const u16*)d_in[10]; const u16* Bim=(const u16*)d_in[11];
  const u16* C2re=(const u16*)d_in[12]; const u16* C2im=(const u16*)d_in[13]; const u16* logdt2=(const u16*)d_in[14]; const u16* Dsk2=(const u16*)d_in[15]; const u16* gw=(const u16*)d_in[16];
  char* ws=(char*)d_ws; float* K=(float*)ws; float* a=(float*)(ws+(8<<20)); float2* Bu=(float2*)(ws+(16<<20));
  k_kgen<<<D_,256,0,stream>>>(logA,Aim,Cre,Cim,logdt,K);
  k_conv<<<D_,256,0,stream>>>(u,K,Dsk,a);
  k_gemm1<<<L_,256,0,stream>>>(u,Bre,Bim,Lre,Lim,logdt2,Bu);
  k_scan<<<(D_+63)/64,64,0,stream>>>(Lre,Lim,logdt2,Bu);
  k_out<<<L_,256,0,stream>>>(u,gw,Bu,C2re,C2im,Dsk2,a,(float*)d_out);
}
```

```cpp
#include <hip/hip_runtime.h>
#include <stdint.h>

typedef unsigned short u16;
typedef unsigned u32;
typedef unsigned long long u64;
typedef float f2 __attribute__((ext_vector_type(2)));
typedef float f4 __attribute__((ext_vector_type(4)));
typedef u32 v2u __attribute__((ext_vector_type(2)));
typedef u32 v4u __attribute__((ext_vector_type(4)));
typedef __bf16 bf2 __attribute__((ext_vector_type(2)));
typedef __bf16 bf8 __attribute__((ext_vector_type(8)));
typedef short s4v __attribute__((ext_vector_type(4)));
#define GAS __attribute__((address_space(1)))
#define LAS __attribute__((address_space(3)))
#define DEVI __device__ __forceinline__
#define RLX_AGENT __ATOMIC_RELAXED, __HIP_MEMORY_SCOPE_AGENT

constexpr int L_ = 2048, D_ = 576, N_ = 128;
constexpr u32 WS_E = 0, WS_ERR = 64, WS_TOK = 4096, WS_UFLAG = 8192, WS_CFLAG = 9216, WS_HFLAG = 10240, WS_AFLAG = 12288;
constexpr u32 WS_G = 65536, WS_S = 131072, WS_UT = 1u << 20, WS_AT = 4u << 20, WS_HT = 8u << 20, WS_CT = 16u << 20;
constexpr u32 WS_STAMP = 20u << 20;
constexpr u32 WS_BYTES = 24u << 20;
constexpr u32 LDS_BYTES = 163840;
constexpr u32 KC_STRIDE = 4160, KC_BYTES = 4 * KC_STRIDE;
constexpr u32 UB_BYTES = 4608;
constexpr u32 KD_BYTES = 5136;
constexpr u32 PR_BYTES = 2048;
constexpr u32 B_RING = 0, B_KC = 98304, B_UB = B_KC + 2 * KC_BYTES, B_KD = B_UB + 2 * UB_BYTES, B_PR = B_KD + 2 * KD_BYTES,
              B_LAM = B_PR + 2 * PR_BYTES, B_MISC = B_LAM + 7680;
static_assert(B_MISC + 64 <= LDS_BYTES, "lds");
constexpr u32 B_XH = B_PR, B_XC = B_PR + 768;
constexpr u32 B_AT = 0, B_XT = 12288, B_GV = 24576, B_RED = 25600, RED_ROW = 400, RED_Q = 64 * RED_ROW;
static_assert(B_RED + 4 * RED_Q <= LDS_BYTES, "lds");
constexpr u32 S_X = 0, S_ST = 36864, S_KC = 73728, S_UB = S_KC + 3 * KC_BYTES, S_KD = S_UB + 3 * UB_BYTES, S_PR = S_KD + 3 * KD_BYTES,
              S_MISC = S_PR + 3 * PR_BYTES, S_GW = S_MISC + 64;
static_assert(S_GW + 1152 <= LDS_BYTES, "lds");

struct Params {
  const u16 *u, *logA, *Aim, *Cre, *Cim, *logdt, *Dsk, *Lre, *Lim, *Bre, *Bim, *C2re, *C2im, *logdt2, *Dsk2, *gw;
  float* out;
  char* ws;
};

struct Tbl { u16 v[2064]; };
constexpr Tbl make_tbl() {
  Tbl t{};
  for (int x = 0; x < 2064; ++x) {
    int l = 2047 - x;
    int dw = 1281;
    if (l >= 0) {
      int e = 0;
      while ((l >> e) >= 256) ++e;
      int pos = l;
      if (e > 0) {
        int m = l >> e, rem = l & ((1 << e) - 1), half = 1 << (e - 1);
        if (rem > half || (rem == half && (m & 1))) ++m;
        pos = m << e;
      }
      if (pos == 2048) dw = 1280;
      else {
        int seg = pos < 256 ? 0 : pos < 512 ? 1 : pos < 1024 ? 2 : 3;
        int q = pos >> seg, a = q >> 4, b = q & 15;
        dw = seg * 320 + (b >> 2) * 80 + (b & 3) * 16 + a;
      }
    }
    t.v[x] = (u16)(dw * 4);
  }
  return t;
}
__device__ const Tbl g_tbl = make_tbl();

DEVI float bf2f(u32 v) { return __uint_as_float(v << 16); }
DEVI float bflo(u32 v) { return __uint_as_float(v << 16); }
DEVI float bfhi(u32 v) { return __uint_as_float(v & 0xffff0000u); }
DEVI u32 pk2(float a, float b) { bf2 t = {(__bf16)a, (__bf16)b}; return __builtin_bit_cast(u32, t); }
DEVI float rbf(float a) { return bflo(pk2(a, a)); }
DEVI f4 mfma16(v4u a, v4u b, f4 c) { return __builtin_amdgcn_mfma_f32_16x16x32_bf16(__builtin_bit_cast(bf8, a), __builtin_bit_cast(bf8, b), c, 0, 0, 0); }
DEVI int rfl(int v) { return __builtin_amdgcn_readfirstlane(v); }
DEVI void barrier() { asm volatile("s_waitcnt lgkmcnt(0)\n\ts_barrier" ::: "memory"); }
DEVI void vm0() { asm volatile("s_waitcnt vmcnt(0)" ::: "memory"); }
template <int N> DEVI void vmw() { asm volatile("s_waitcnt vmcnt(%0)" ::"n"(N) : "memory"); }
DEVI void dma(u32 voff, const void* sbase, u32 dst) {
  u32 keep;
  asm volatile("s_mov_b32 %0, m0\n\ts_mov_b32 m0, %3\n\ts_nop 0\n\tglobal_load_lds_dwordx4 %1, %2\n\ts_mov_b32 m0, %0"
               : "=&s"(keep) : "v"(voff), "s"(sbase), "s"(dst) : "memory");
}
DEVI void dma_sc1(u32 voff, const void* sbase, u32 dst) {
  u32 keep;
  asm volatile("s_mov_b32 %0, m0\n\ts_mov_b32 m0, %3\n\ts_nop 0\n\tglobal_load_lds_dwordx4 %1, %2 sc1\n\ts_mov_b32 m0, %0"
               : "=&s"(keep) : "v"(voff), "s"(sbase), "s"(dst) : "memory");
}
DEVI v4u lds128(LAS char* sm, u32 off) { return *(LAS v4u*)(sm + off); }
DEVI v2u lds64(LAS char* sm, u32 off) { return *(LAS v2u*)(sm + off); }
DEVI float ldsf(LAS char* sm, u32 off) { return *(LAS float*)(sm + off); }
DEVI void stl128(LAS char* sm, u32 off, v4u v) { *(LAS v4u*)(sm + off) = v; }
DEVI void stl64(LAS char* sm, u32 off, v2u v) { *(LAS v2u*)(sm + off) = v; }
DEVI void stlf(LAS char* sm, u32 off, float v) { *(LAS float*)(sm + off) = v; }
DEVI v2u ldstr(LAS char* sm, u32 off) { return __builtin_bit_cast(v2u, __builtin_amdgcn_ds_read_tr16_b64_v4i16((LAS s4v*)(sm + off))); }
DEVI u64 ldg64(const char* p) { return __hip_atomic_load((GAS u64*)p, RLX_AGENT); }
DEVI void stg64(char* p, u64 v) { __hip_atomic_store((GAS u64*)p, v, RLX_AGENT); }
DEVI float ldgf(const char* p) { return __uint_as_float(__hip_atomic_load((GAS u32*)p, RLX_AGENT)); }
DEVI void stgf(char* p, float v) { __hip_atomic_store((GAS u32*)p, __float_as_uint(v), RLX_AGENT); }
DEVI u32 ldu16(const u16* p) { return *(const GAS u16*)p; }

DEVI f2 csq(f2 s) {
  f2 p, r;
  asm("v_pk_mul_f32 %0, %1, %1 op_sel_hi:[0,1]" : "=v"(p) : "v"(s));
  asm("v_pk_fma_f32 %0, %1, %1, %2 op_sel:[1,1,0] op_sel_hi:[0,1,1] neg_lo:[1,0,0]" : "=v"(r) : "v"(s), "v"(p));
  return r;
}
DEVI f2 cmul(f2 a, f2 b) {
  f2 p, r;
  asm("v_pk_mul_f32 %0, %1, %2 op_sel_hi:[0,1]" : "=v"(p) : "v"(a), "v"(b));
  asm("v_pk_fma_f32 %0, %1, %2, %3 op_sel:[1,1,0] op_sel_hi:[1,0,1] neg_lo:[1,0,0]" : "=v"(r) : "v"(a), "v"(b), "v"(p));
  return r;
}
DEVI f2 cmulc(f2 a, f2 b) {
  f2 p, r;
  asm("v_pk_mul_f32 %0, %1, %2 op_sel_hi:[0,1] neg_hi:[0,1]" : "=v"(p) : "v"(a), "v"(b));
  asm("v_pk_fma_f32 %0, %1, %2, %3 op_sel:[1,1,0] op_sel_hi:[1,0,1] neg_lo:[1,0,0] neg_hi:[1,0,0]" : "=v"(r) : "v"(a), "v"(b), "v"(p));
  return r;
}
DEVI float swap8(float h) { return __int_as_float(__builtin_amdgcn_update_dpp(0, __float_as_int(h), 0x128, 0xf, 0xf, true)); }
DEVI float lmul_add(float a, float s, float h, float g) { return __builtin_fmaf(s, swap8(h), __builtin_fmaf(a, h, g)); }

constexpr u32 SPIN_MAX = 1u << 18;
DEVI void spin_fail(char* ws, u32 code) { __hip_atomic_store((GAS u32*)(ws + WS_ERR), code, RLX_AGENT); }
DEVI void wait_flags(char* ws, u32 off, int n, u64 tag, int l, u32 code) {
  const char* p = ws + off + (l < n ? l : 0) * 8;
  for (u32 s = 0;; ++s) {
    u64 v = ldg64(p);
    if (__all(v == tag)) break;
    if (s > SPIN_MAX) { if (l == 0) spin_fail(ws, code); break; }
    __builtin_amdgcn_s_sleep(1);
  }
}
DEVI void stamp(char*, int, int, int, int) {}

DEVI void a_params(const Params& P, LAS char* sm, int ch, int n, u32 pr, u32 kd, int l) {
  const int i = ch * N_ + n;
  const float la = bf2f(ldu16(P.logA + i)), ai = bf2f(ldu16(P.Aim + i)), cr = bf2f(ldu16(P.Cre + i)), ci = bf2f(ldu16(P.Cim + i));
  const float dt = rbf(expf(bf2f(ldu16(P.logdt + ch))));
  const float are = -rbf(expf(la));
  const float ar2 = (are * dt) * 1.4426950408889634f, aiv = (ai * dt) * 0.15915494309189535f;
  const float c2r = 2.f * (cr * dt), c2i = 2.f * (ci * dt);
  v4u pv = {__float_as_uint(ar2), __float_as_uint(aiv), __float_as_uint(c2r), __float_as_uint(c2i)};
  stl128(sm, pr + n * 16, pv);
  const float mag = __builtin_amdgcn_exp2f(ar2 * 2048.f), th = __builtin_amdgcn_fractf(aiv * 2048.f);
  float k = mag * (c2r * __builtin_amdgcn_cosf(th) - c2i * __builtin_amdgcn_sinf(th));
#pragma unroll
  for (int o = 32; o > 0; o >>= 1) k += __shfl_xor(k, o);
  if (l == 0) __hip_atomic_fetch_add((LAS float*)(sm + kd + 1280 * 4), k, __ATOMIC_RELAXED, __HIP_MEMORY_SCOPE_WORKGROUP);
}
DEVI void kgen_task(LAS char* sm, u32 pr, int kappa, int l, f4 (&acc)[4]) {
  const float x = (float)(l & 15);
  v4u V[4], W[4];
#pragma unroll
  for (int i = 0; i < 4; ++i) {
    const v4u pv = lds128(sm, pr + (16 * kappa + 4 * (l >> 4) + i) * 16);
    const float mag = __builtin_amdgcn_exp2f(x * __uint_as_float(pv[0]));
    const float th = __builtin_amdgcn_fractf(x * __uint_as_float(pv[1]));
    f2 s = {mag * __builtin_amdgcn_cosf(th), mag * __builtin_amdgcn_sinf(th)};
    const f2 c = {__uint_as_float(pv[2]), __uint_as_float(pv[3])};
#pragma unroll
    for (int k = 0; k < 4; ++k) { V[k][i] = pk2(s.x, s.y); s = csq(s); }
#pragma unroll
    for (int k = 0; k < 4; ++k) { const f2 wv = cmulc(c, s); W[k][i] = pk2(wv.x, wv.y); if (k < 3) s = csq(s); }
  }
#pragma unroll
  for (int k = 0; k < 4; ++k) acc[k] = mfma16(V[k], W[k], acc[k]);
}
DEVI void kgen_flush(LAS char* sm, u32 kd, int l, const f4 (&acc)[4]) {
#pragma unroll
  for (int k = 0; k < 4; ++k)
#pragma unroll
    for (int r = 0; r < 4; ++r)
      __hip_atomic_fetch_add((LAS float*)(sm + kd + (k * 320 + (l >> 4) * 80 + r * 16 + (l & 15)) * 4), acc[k][r], __ATOMIC_RELAXED, __HIP_MEMORY_SCOPE_WORKGROUP);
}
DEVI void expand_chunk(LAS char* sm, u32 kd, u32 kc, int q, v4u ta, v2u tb) {
  float e[11];
#pragma unroll
  for (int i = 0; i < 11; ++i) {
    const u32 wv = i < 8 ? ta[i >> 1] : tb[(i - 8) >> 1];
    e[i] = ldsf(sm, kd + ((i & 1) ? (wv >> 16) : (wv & 0xffffu)));
  }
  u32 Pk[5], Qk[5];
#pragma unroll
  for (int k = 0; k < 5; ++k) { Pk[k] = pk2(e[2 * k], e[2 * k + 1]); Qk[k] = pk2(e[2 * k + 1], e[2 * k + 2]); }
  stl128(sm, kc + 3 * KC_STRIDE + q * 16, (v4u){Pk[0], Pk[1], Pk[2], Pk[3]});
  stl128(sm, kc + 2 * KC_STRIDE + q * 16, (v4u){Qk[0], Qk[1], Qk[2], Qk[3]});
  stl128(sm, kc + 1 * KC_STRIDE + q * 16, (v4u){Pk[1], Pk[2], Pk[3], Pk[4]});
  stl128(sm, kc + 0 * KC_STRIDE + q * 16, (v4u){Qk[1], Qk[2], Qk[3], Qk[4]});
}
DEVI void conv_half(LAS char* sm, u32 kc, u32 ub, int hr, int l, f4 (&acc)[8]) {
  const int r = l & 15, g = l >> 4;
  u32 tb = kc + (r & 3) * KC_STRIDE + 2 * (2028 - 4 * (r >> 2) + 8 * g) - 512 * 7 - 256 * hr;
  u32 ubs = ub + 2 * (256 - 16 + 16 * r + 8 * g) - 256 * hr;
#pragma unroll
  for (int i = 0; i < 8; ++i) acc[i] = (f4){0.f, 0.f, 0.f, 0.f};
#pragma unroll
  for (int it = 0; it < 4; ++it) {
    v4u U[8];
#pragma unroll
    for (int m = 0; m < 8; ++m) U[m] = lds128(sm, ubs + 512 * m - 64 * it);
#pragma unroll
    for (int n = 0; n < 8; ++n) {
      const v2u t0 = lds64(sm, tb + 512 * (7 - n) - 64 * it), t1 = lds64(sm, tb + 512 * (7 - n) - 64 * it + 8);
      const v4u T = {t0[0], t0[1], t1[0], t1[1]};
#pragma unroll
      for (int i = n; i < 8; ++i) acc[i] = mfma16(T, U[i - n], acc[i]);
    }
  }
}
DEVI void conv_finish(LAS char* sm, u32 ub, int i0, int l, const f4 (&y)[4], float dsk, __amdgpu_buffer_rsrc_t rs, int ch) {
#pragma unroll
  for (int i = 0; i < 4; ++i) {
    const u32 o = ub + 512 + 2 * (256 * (i0 + i) + 16 * (l & 15) + 4 * (l >> 4));
    const v2u uu = lds64(sm, o);
    const float a0 = rbf(y[i][0]) + rbf(bflo(uu[0]) * dsk), a1 = rbf(y[i][1]) + rbf(bfhi(uu[0]) * dsk);
    const float a2 = rbf(y[i][2]) + rbf(bflo(uu[1]) * dsk), a3 = rbf(y[i][3]) + rbf(bfhi(uu[1]) * dsk);
    stl64(sm, o, (v2u){pk2(a0, a1), pk2(a2, a3)});
  }
  asm volatile("s_waitcnt lgkmcnt(0)" ::: "memory");
#pragma unroll
  for (int i = 0; i < 2; ++i) {
    const u32 e = 256 * i0 + 512 * i + 8 * l;
    const v4u v = lds128(sm, ub + 512 + 2 * e);
    __builtin_amdgcn_raw_buffer_store_b128(v, rs, WS_AT + (u32)ch * 4096u + 2 * e, 0, 16);
  }
}

DEVI void spare_wg(const Params& P, LAS char* sm, int sidx, int b, int w, int l, int tid) {
  char* ws = P.ws;
  const u32 sb = (u32)(size_t)sm;
  const __amdgpu_buffer_rsrc_t rs = __builtin_amdgcn_make_buffer_rsrc(ws, 0, WS_BYTES, 0x00020000);
  {
    const char* xs = (const char*)P.u + (size_t)sidx * 36864;
#pragma unroll
    for (int i = 0; i < 5; ++i) {
      const int pc = w + 8 * i;
      if (pc < 36) dma((u32)(pc * 1024 + l * 16), xs, sb + S_X + pc * 1024);
    }
  }
  if (tid < 72) stl128(sm, S_GW + tid * 16, *(const GAS v4u*)((const char*)P.gw + tid * 16));
  u64 tag = 0;
  if (tid == 0) {
    tag = ldg64(ws + WS_E) + 1;
    *(LAS u64*)(sm + S_MISC) = tag;
    stg64(ws + WS_TOK + b * 8, tag);
  }
  for (int i = tid; i < 3 * (int)(KD_BYTES / 4); i += 512) stlf(sm, S_KD + i * 4, 0.f);
  if (tid < 96) stl128(sm, S_UB + (tid >> 5) * UB_BYTES + (tid & 31) * 16, (v4u){0, 0, 0, 0});
  if (tid < 24) stl128(sm, S_KC + (tid >> 3) * KC_BYTES + ((tid >> 1) & 3) * KC_STRIDE + 4096 + (tid & 1) * 16, (v4u){0, 0, 0, 0});
  vm0();
  barrier();
  tag = *(LAS u64*)(sm + S_MISC);
  stamp(ws, b, w, l, 1);
  {
    const int g = l >> 4, q = (l & 15) >> 2, p = l & 3;
#pragma unroll
    for (int i = 0; i < 5; ++i) {
      const int cb = w + 8 * i;
      if (cb < 36) {
        const u32 a0 = S_X + (8 * g + q) * 1152 + (cb * 16 + 4 * p) * 2;
        const v2u lo = ldstr(sm, a0), hi = ldstr(sm, a0 + 4 * 1152);
        stl128(sm, S_ST + (cb * 16 + (l & 15)) * 64 + g * 16, (v4u){lo[0], lo[1], hi[0], hi[1]});
      }
    }
  }
  if (w >= 4) {
    const int row = 8 * (w - 4) + (l >> 3), q = l & 7;
    float s = 0.f;
#pragma unroll
    for (int i = 0; i < 9; ++i) {
      const v4u xv = lds128(sm, S_X + row * 1152 + (q + 8 * i) * 16), wv = lds128(sm, S_GW + (q + 8 * i) * 16);
#pragma unroll
      for (int k = 0; k < 4; ++k) {
        const u32 pr = pk2(bflo(xv[k]) * bflo(wv[k]), bfhi(xv[k]) * bfhi(wv[k]));
        s += bflo(pr) + bfhi(pr);
      }
    }
    s += __shfl_xor(s, 1); s += __shfl_xor(s, 2); s += __shfl_xor(s, 4);
    const float gv = rbf(1.f / (1.f + expf(-rbf(s))));
    if (q == 0) stgf(ws + WS_G + (sidx * 32 + row) * 4, gv);
  }
  barrier();
#pragma unroll
  for (int i = 0; i < 5; ++i) {
    const int pc = w + 8 * i;
    if (pc < 36) {
      const v4u v = lds128(sm, S_ST + pc * 1024 + l * 16);
      __builtin_amdgcn_raw_buffer_store_b128(v, rs, WS_UT + (u32)(pc * 16 + (l >> 2)) * 4096u + sidx * 64 + (l & 3) * 16, 0, 16);
    }
  }
  vm0();
  barrier();
  if (tid == 0) stg64(ws + WS_UFLAG + sidx * 8, tag);
  stamp(ws, b, w, l, 2);
  {
    const int row = l >> 2, cc = l & 3;
#pragma unroll
    for (int i = 0; i < 3; ++i) {
      const int fl = w + 8 * i, f = sidx * 21 + fl;
      if (fl < 21 && f < 1296) {
        const int dt = f % 3, ii = (f / 3) % 9, kq = (f / 27) & 3, dd = f / 108;
        const int d = dd * 48 + dt * 16 + row, pp = 288 * (kq & 1) + 32 * ii + 8 * (cc ^ ((row >> 1) & 3));
        dma((u32)(d * 1152 + pp * 2), (kq >> 1) ? P.C2im : P.C2re, sb + S_ST + fl * 1024);
      }
    }
    vm0();
#pragma unroll
    for (int i = 0; i < 3; ++i) {
      const int fl = w + 8 * i, f = sidx * 21 + fl;
      if (fl < 21 && f < 1296) {
        const v4u v = lds128(sm, S_ST + fl * 1024 + (l & 15) * 64 + 16 * ((l >> 4) ^ (((l & 15) >> 1) & 3)));
        __builtin_amdgcn_raw_buffer_store_b128(v, rs, WS_CT + (u32)f * 1024u + l * 16, 0, 16);
      }
    }
    vm0();
    barrier();
    if (tid == 0) stg64(ws + WS_CFLAG + sidx * 8, tag);
  }
  stamp(ws, b, w, l, 3);
  const int ch0 = 384 + 3 * sidx;
  if (tid < 384) a_params(P, sm, ch0 + (tid >> 7), tid & 127, S_PR + (tid >> 7) * PR_BYTES, S_KD + (tid >> 7) * KD_BYTES, l);
  v4u ta = *(const GAS v4u*)((const char*)g_tbl.v + (tid & 255) * 16);
  v2u tb2 = *(const GAS v2u*)((const char*)g_tbl.v + (tid & 255) * 16 + 16);
  barrier();
#pragma unroll 1
  for (int c3 = 0; c3 < 3; ++c3) {
    f4 acc[4] = {};
    kgen_task(sm, S_PR + c3 * PR_BYTES, w, l, acc);
    kgen_flush(sm, S_KD + c3 * KD_BYTES, l, acc);
  }
  barrier();
  expand_chunk(sm, S_KD + (tid >> 8) * KD_BYTES, S_KC + (tid >> 8) * KC_BYTES, tid & 255, ta, tb2);
  if (tid < 256) expand_chunk(sm, S_KD + 2 * KD_BYTES, S_KC + 2 * KC_BYTES, tid, ta, tb2);
  stamp(ws, b, w, l, 4);
  f4 acc[8];
  const int c3 = w % 3, hr = w / 3;
  if (w < 6) {
    wait_flags(ws, WS_UFLAG, 64, tag, l, 1);
#pragma unroll
    for (int i = 0; i < 4; ++i) dma_sc1((u32)(i * 1024 + l * 16), ws + WS_UT + (size_t)(ch0 + c3) * 4096, sb + S_UB + c3 * UB_BYTES + 512 + i * 1024);
    vm0();
  }
  barrier();
  stamp(ws, b, w, l, 5);
  if (w < 6) {
    conv_half(sm, S_KC + c3 * KC_BYTES, S_UB + c3 * UB_BYTES, hr, l, acc);
    if (hr) {
#pragma unroll
      for (int i = 0; i < 4; ++i) stl128(sm, S_X + (w * 4 + i) * 1024 + l * 16, __builtin_bit_cast(v4u, acc[i]));
    } else {
#pragma unroll
      for (int i = 0; i < 4; ++i) stl128(sm, S_X + (w * 4 + i) * 1024 + l * 16, __builtin_bit_cast(v4u, acc[4 + i]));
    }
  }
  barrier();
  if (w < 6) {
    const int pw = hr ? w - 3 : w + 3;
    f4 y[4];
    if (hr) {
#pragma unroll
      for (int i = 0; i < 4; ++i) y[i] = acc[4 + i] + __builtin_bit_cast(f4, lds128(sm, S_X + (pw * 4 + i) * 1024 + l * 16));
    } else {
#pragma unroll
      for (int i = 0; i < 4; ++i) y[i] = acc[i] + __builtin_bit_cast(f4, lds128(sm, S_X + (pw * 4 + i) * 1024 + l * 16));
    }
    conv_finish(sm, S_UB + c3 * UB_BYTES, 4 * hr, l, y, bf2f(ldu16(P.Dsk + ch0 + c3)), rs, ch0 + c3);
    vm0();
  }
  barrier();
  if (tid < 3) stg64(ws + WS_AFLAG + (ch0 + tid) * 8, tag);
  stamp(ws, b, w, l, 6);
}

struct LC { float a, s; };
DEVI LC lamc(LAS char* sm, int pl, int k, int comp) {
  const v2u v = lds64(sm, B_LAM + pl * 80 + k * 8);
  const float zi = __uint_as_float(v[1]);
  return LC{__uint_as_float(v[0]), comp ? zi : -zi};
}

DEVI void b_wg(const Params& P, LAS char* sm, int c, int j, int b, int w, int l, int tid) {
  char* ws = P.ws;
  const u32 sb = (u32)(size_t)sm;
  const __amdgpu_buffer_rsrc_t rs = __builtin_amdgcn_make_buffer_rsrc(ws, 0, WS_BYTES, 0x00020000);
  const int bw = c * 6 + j, ch0 = 2 * bw;
  const int rot = ((c & 3) * 2 + (j % 3) * 3 + (j / 3)) % 9;
  u32 vx[2], vb[3];
  {
    const int lw = w & 3, rr = l >> 3;
#pragma unroll
    for (int i = 0; i < 2; ++i) {
      const int t = 16 * lw + 8 * i + rr, f = 2 * ((t >> 3) & 3) + ((t >> 1) & 1);
      vx[i] = (u32)((c * 64 + t) * 1152 + 16 * ((l & 7) ^ f));
    }
#pragma unroll
    for (int i = 0; i < 3; ++i) {
      const int p = 96 * j + 8 * (3 * lw + i) + rr;
      vb[i] = (u32)(p * 1152);
    }
  }
  auto issue = [&](int s) {
    int ks = s + rot; if (ks >= 9) ks -= 9;
    const u32 slot = sb + B_RING + (s % 3) * 32768;
    const int lw = w & 3, rr = l >> 3;
    const char* xb = (const char*)P.u + ks * 128;
#pragma unroll
    for (int i = 0; i < 2; ++i) dma(vx[i], xb, slot + (2 * lw + i) * 1024);
    const char* br = (const char*)P.Bre + ks * 128;
    const char* bi = (const char*)P.Bim + ks * 128;
#pragma unroll
    for (int i = 0; i < 3; ++i) {
      dma(vb[i] + 16 * ((l & 7) ^ (rr >> 1)), br, slot + 8192 + (6 * lw + 2 * i) * 1024);
      dma(vb[i] + 16 * ((l & 7) ^ (4 + (rr >> 1))), bi, slot + 8192 + (6 * lw + 2 * i + 1) * 1024);
    }
  };
  if (w >= 4) { issue(0); issue(1); }
  float lre = 0.f, lim = 0.f, ldt = 0.f;
  if (w < 4) {
    if (tid < 96) { const int p = 96 * j + tid; lre = bf2f(ldu16(P.Lre + p)); lim = bf2f(ldu16(P.Lim + p)); ldt = bf2f(ldu16(P.logdt2 + p)); }
    if (tid == 0) {
      const u64 tg = ldg64(ws + WS_E) + 1;
      *(LAS u64*)(sm + B_MISC) = tg;
      stg64(ws + WS_TOK + b * 8, tg);
    }
  }
  for (int i = tid; i < 2 * (int)(KD_BYTES / 4); i += 512) stlf(sm, B_KD + i * 4, 0.f);
  if (tid < 64) stl128(sm, B_UB + (tid >> 5) * UB_BYTES + (tid & 31) * 16, (v4u){0, 0, 0, 0});
  if (tid < 16) stl128(sm, B_KC + (tid >> 3) * KC_BYTES + ((tid >> 1) & 3) * KC_STRIDE + 4096 + (tid & 1) * 16, (v4u){0, 0, 0, 0});
  barrier();
  if (w < 4) a_params(P, sm, ch0 + (tid >> 7), tid & 127, B_PR + (tid >> 7) * PR_BYTES, B_KD + (tid >> 7) * KD_BYTES, l);
  barrier();
  const u64 tag = *(LAS u64*)(sm + B_MISC);
  stamp(ws, b, w, l, 1);
  {
    f4 acc[4] = {};
#pragma unroll 1
    for (int k = 0; k < 2; ++k) kgen_task(sm, B_PR + (w & 1) * PR_BYTES, 2 * (w >> 1) + k, l, acc);
    kgen_flush(sm, B_KD + (w & 1) * KD_BYTES, l, acc);
  }
  v4u ta; v2u tb2;
  if (w < 4) {
    ta = *(const GAS v4u*)((const char*)g_tbl.v + (tid & 255) * 16);
    tb2 = *(const GAS v2u*)((const char*)g_tbl.v + (tid & 255) * 16 + 16);
  }
  barrier();
  if (w < 4) {
    expand_chunk(sm, B_KD, B_KC, tid, ta, tb2);
    expand_chunk(sm, B_KD + KD_BYTES, B_KC + KC_BYTES, tid, ta, tb2);
    if (tid < 96) {
      const float dt = rbf(expf(ldt));
      const float e = expf(dt * lre);
      float sn, cs; sincosf(dt * lim, &sn, &cs);
      const f2 lam = {e * cs, e * sn};
      const float nr = lam.x - 1.f, ni = lam.y, den = lre * lre + lim * lim;
      const f2 coef = {(nr * lre + ni * lim) / den, (ni * lre - nr * lim) / den};
      f2 z8 = csq(csq(csq(lam))), z16 = csq(z8), z24 = cmul(z16, z8), z32 = csq(z16), mu = csq(z32);
      f2 m8 = csq(csq(csq(mu))), m16 = csq(m8), m24 = cmul(m16, m8);
      LAS f2* t = (LAS f2*)(sm + B_LAM + tid * 80);
      t[0] = lam; t[1] = z8; t[2] = z16; t[3] = z24; t[4] = z32; t[5] = mu; t[6] = m8; t[7] = m16; t[8] = m24; t[9] = coef;
    }
  }
  stamp(ws, b, w, l, 2);
  const int cg = w & 3, mh = w >> 2, x = l & 15, G = l >> 4;
  f4 g1[2][3] = {};
  {
    const int t0 = 32 * mh + 8 * (x >> 2) + (x & 3), f = 2 * ((t0 >> 3) & 3) + ((t0 >> 1) & 1);
    const u32 aa0 = t0 * 128 + 16 * (G ^ f), aa1 = aa0 ^ 64;
    const u32 bb0 = 8192 + (48 * cg + x) * 128 + 16 * (G ^ (x >> 1)), bb1 = bb0 ^ 64;
#pragma unroll
    for (int s = 0; s < 9; ++s) {
      if (w >= 4) { if (s < 8) vmw<8>(); else vm0(); }
      barrier();
      if (w >= 4 && s + 2 < 9) issue(s + 2);
      const u32 so = B_RING + (s % 3) * 32768;
#pragma unroll
      for (int ks = 0; ks < 2; ++ks) {
        v4u A[2], B[3];
#pragma unroll
        for (int mt = 0; mt < 2; ++mt) A[mt] = lds128(sm, so + (ks ? aa1 : aa0) + 512 * mt);
#pragma unroll
        for (int nt = 0; nt < 3; ++nt) B[nt] = lds128(sm, so + (ks ? bb1 : bb0) + 2048 * nt);
#pragma unroll
        for (int mt = 0; mt < 2; ++mt)
#pragma unroll
          for (int nt = 0; nt < 3; ++nt) g1[mt][nt] = mfma16(A[mt], B[nt], g1[mt][nt]);
      }
    }
  }
  stamp(ws, b, w, l, 3);
  const int comp = x >> 3;
  float X[3], Hh[3];
  {
    LC l1[3], l8[3], l16[3];
#pragma unroll
    for (int nt = 0; nt < 3; ++nt) { const int pl = 24 * cg + 8 * nt + (x & 7); l1[nt] = lamc(sm, pl, 0, comp); l8[nt] = lamc(sm, pl, 1, comp); l16[nt] = lamc(sm, pl, 2, comp); }
#pragma unroll
    for (int nt = 0; nt < 3; ++nt) {
      float h = g1[0][nt][0];
#pragma unroll
      for (int tau = 1; tau < 8; ++tau) h = lmul_add(l1[nt].a, l1[nt].s, h, g1[tau >> 2][nt][tau & 3]);
      const float v1 = __uint_as_float(__builtin_amdgcn_permlane16_swap(__float_as_uint(h), __float_as_uint(h), false, false)[0]);
      const bool odd = G & 1;
      h = lmul_add(odd ? l8[nt].a : 0.f, odd ? l8[nt].s : 0.f, v1, h);
      const u32 r1 = __builtin_amdgcn_permlane16_swap(__float_as_uint(h), __float_as_uint(h), false, false)[1];
      const float v2 = __uint_as_float(__builtin_amdgcn_permlane32_swap(r1, r1, false, false)[0]);
      const float ma = G == 2 ? l8[nt].a : G == 3 ? l16[nt].a : 0.f, ms = G == 2 ? l8[nt].s : G == 3 ? l16[nt].s : 0.f;
      h = lmul_add(ma, ms, v2, h);
      const float v3 = __uint_as_float(__builtin_amdgcn_permlane16_swap(__float_as_uint(h), __float_as_uint(h), false, false)[0]);
      X[nt] = G == 0 ? 0.f : G == 2 ? v2 : v3;
      Hh[nt] = h;
    }
  }
  if (mh == 0 && G == 3) {
#pragma unroll
    for (int nt = 0; nt < 3; ++nt) stlf(sm, B_XH + ((cg * 3 + nt) * 16 + x) * 4, Hh[nt]);
  }
  barrier();
  f4 cacc[8];
  float H0[3], carry[3];
  if (mh == 1) {
    const u32 taglo = (u32)tag, taghi = (u32)(tag >> 32) & 0xffu;
#pragma unroll
    for (int nt = 0; nt < 3; ++nt) {
      const int pl = 24 * cg + 8 * nt + (x & 7);
      H0[nt] = ldsf(sm, B_XH + ((cg * 3 + nt) * 16 + x) * 4);
      const LC l32 = lamc(sm, pl, 4, comp);
      const float S = lmul_add(l32.a, l32.s, H0[nt], Hh[nt]);
      if (G == 3) {
        const u32 hi = ((__float_as_uint(S) + 0x80u) & 0xffffff00u) | taghi;
        stg64(ws + WS_S + ((c * 72 + 12 * j + 3 * cg + nt) * 16 + x) * 8, ((u64)hi << 32) | taglo);
      }
    }
    stamp(ws, b, w, l, 4);
    float val[3][8];
    if (c > 0) {
      for (u32 sp = 0;; ++sp) {
        bool ok = true;
#pragma unroll
        for (int i = 0; i < 8; ++i) {
          const int cp = c - 32 + 8 * G + i;
          const bool use = cp >= 0;
#pragma unroll
          for (int nt = 0; nt < 3; ++nt) {
            const u64 v = ldg64(ws + WS_S + (((use ? cp : 0) * 72 + 12 * j + 3 * cg + nt) * 16 + x) * 8);
            const u32 hi = (u32)(v >> 32);
            ok &= !use || ((u32)v == taglo && (hi & 0xffu) == taghi);
            val[nt][i] = use ? __uint_as_float(hi & 0xffffff00u) : 0.f;
          }
        }
        if (__all(ok)) break;
        if (sp > SPIN_MAX) { if (l == 0) spin_fail(ws, 2); break; }
        __builtin_amdgcn_s_sleep(1);
      }
    } else {
#pragma unroll
      for (int nt = 0; nt < 3; ++nt)
#pragma unroll
        for (int i = 0; i < 8; ++i) val[nt][i] = 0.f;
    }
    stamp(ws, b, w, l, 5);
#pragma unroll
    for (int nt = 0; nt < 3; ++nt) {
      const int pl = 24 * cg + 8 * nt + (x & 7);
      const LC mu = lamc(sm, pl, 5, comp);
      float a = val[nt][0];
#pragma unroll
      for (int i = 1; i < 8; ++i) a = lmul_add(mu.a, mu.s, a, val[nt][i]);
      const LC mg = lamc(sm, pl, G == 3 ? 6 : 8 - G, comp);
      const float sc = lmul_add(mg.a, mg.s, a, 0.f);
      a = G == 3 ? a : sc;
      auto r1 = __builtin_amdgcn_permlane16_swap(__float_as_uint(a), __float_as_uint(a), false, false);
      a = __uint_as_float(r1[0]) + __uint_as_float(r1[1]);
      auto r2 = __builtin_amdgcn_permlane32_swap(__float_as_uint(a), __float_as_uint(a), false, false);
      carry[nt] = __uint_as_float(r2[0]) + __uint_as_float(r2[1]);
      if (G == 0) stlf(sm, B_XC + ((cg * 3 + nt) * 16 + x) * 4, carry[nt]);
    }
  } else {
    wait_flags(ws, WS_UFLAG, 64, tag, l, 3);
#pragma unroll
    for (int i = 0; i < 4; ++i) dma_sc1((u32)(i * 1024 + l * 16), ws + WS_UT + (size_t)(ch0 + (w & 1)) * 4096, sb + B_UB + (w & 1) * UB_BYTES + 512 + i * 1024);
    vm0();
    stamp(ws, b, w, l, 4);
    conv_half(sm, B_KC + (w & 1) * KC_BYTES, B_UB + (w & 1) * UB_BYTES, w >> 1, l, cacc);
    stamp(ws, b, w, l, 5);
  }
  barrier();
  if (mh == 0) {
    const int hr = w >> 1;
    if (hr) {
#pragma unroll
      for (int i = 0; i < 4; ++i) stl128(sm, B_KC + (w * 4 + i) * 1024 + l * 16, __builtin_bit_cast(v4u, cacc[i]));
    } else {
#pragma unroll
      for (int i = 0; i < 4; ++i) stl128(sm, B_KC + (w * 4 + i) * 1024 + l * 16, __builtin_bit_cast(v4u, cacc[4 + i]));
    }
  }
  {
#pragma unroll
    for (int nt = 0; nt < 3; ++nt) {
      const int pl = 24 * cg + 8 * nt + (x & 7);
      float hc;
      if (mh == 0) hc = ldsf(sm, B_XC + ((cg * 3 + nt) * 16 + x) * 4);
      else { const LC l32 = lamc(sm, pl, 4, comp); hc = lmul_add(l32.a, l32.s, carry[nt], H0[nt]); }
      const LC lg = lamc(sm, pl, G == 0 ? 0 : G, comp);
      const float sc = lmul_add(lg.a, lg.s, hc, X[nt]);
      float h = G == 0 ? hc : sc;
      const LC l1 = lamc(sm, pl, 0, comp);
      LC cf = lamc(sm, pl, 9, comp);
      if (comp) { cf.a = -cf.a; cf.s = -cf.s; }
      float o[8];
#pragma unroll
      for (int tau = 0; tau < 8; ++tau) {
        h = lmul_add(l1.a, l1.s, h, g1[tau >> 2][nt][tau & 3]);
        o[tau] = __builtin_fmaf(cf.s, swap8(h), cf.a * h);
      }
      const int pg = 12 * j + 3 * cg + nt, jj = x & 7;
      const int pos = (jj & 3) | ((G & 1) << 2) | (((pg & 1) ^ comp) << 3) | ((jj >> 2) << 4) | ((G >> 1) << 5);
      const v4u v = {pk2(o[0], o[1]), pk2(o[2], o[3]), pk2(o[4], o[5]), pk2(o[6], o[7])};
      __builtin_amdgcn_raw_buffer_store_b128(v, rs, WS_HT + (u32)((c * 72 + pg) * 2 + mh) * 1024u + pos * 16, 0, 16);
    }
  }
  vm0();
  barrier();
  if (tid == 0) stg64(ws + WS_HFLAG + bw * 8, tag);
  stamp(ws, b, w, l, 6);
  if (mh == 0) {
    const int hr = w >> 1, pw = w ^ 2;
    f4 y[4];
    if (hr) {
#pragma unroll
      for (int i = 0; i < 4; ++i) y[i] = cacc[4 + i] + __builtin_bit_cast(f4, lds128(sm, B_KC + (pw * 4 + i) * 1024 + l * 16));
    } else {
#pragma unroll
      for (int i = 0; i < 4; ++i) y[i] = cacc[i] + __builtin_bit_cast(f4, lds128(sm, B_KC + (pw * 4 + i) * 1024 + l * 16));
    }
    conv_finish(sm, B_UB + (w & 1) * UB_BYTES, 4 * hr, l, y, bf2f(ldu16(P.Dsk + ch0 + (w & 1))), rs, ch0 + (w & 1));
    vm0();
  }
  barrier();
  if (tid < 2) stg64(ws + WS_AFLAG + (ch0 + tid) * 8, tag);
  stamp(ws, b, w, l, 7);
  const int dh = w & 1, kq = w >> 1;
  v4u Cf[9][3];
  wait_flags(ws, WS_CFLAG, 62, tag, l, 4);
  {
    const char* cb = ws + WS_CT + (size_t)((((j * 2 + dh) * 4 + kq) * 27)) * 1024 + l * 16;
#pragma unroll
    for (int i = 0; i < 9; ++i)
#pragma unroll
      for (int dt = 0; dt < 3; ++dt) {
        const GAS u64* q = (const GAS u64*)(cb + (i * 3 + dt) * 1024);
        const u64 lo = __hip_atomic_load(q, RLX_AGENT), hi = __hip_atomic_load(q + 1, RLX_AGENT);
        Cf[i][dt] = (v4u){(u32)lo, (u32)(lo >> 32), (u32)hi, (u32)(hi >> 32)};
      }
  }
  vm0();
#pragma unroll
  for (int i = 0; i < 9; ++i)
#pragma unroll
    for (int dt = 0; dt < 3; ++dt) asm volatile("" : "+v"(Cf[i][dt]));
  stamp(ws, b, w, l, 8);
  if (w == 0) wait_flags(ws, WS_HFLAG + c * 48, 6, tag, l, 5);
  barrier();
  stamp(ws, b, w, l, 9);
#pragma unroll
  for (int be = 0; be < 3; ++be)
#pragma unroll
    for (int i = 0; i < 6; ++i) {
      const int pc = w * 6 + i, half = pc / 24, q = pc % 24;
      dma_sc1((u32)(l * 16), ws + WS_HT + (size_t)((c * 72 + 12 * (be + 3 * half)) * 2 + q) * 1024, sb + be * 49152 + pc * 1024);
    }
  f4 o2[3][4] = {};
  {
    const int cmp = kq >> 1, hf = kq & 1;
    const int q = (l & 15) >> 2, p = l & 3;
    u32 ta2[2];
#pragma unroll
    for (int t = 0; t < 2; ++t) {
      const int jj = 4 * t + q;
      const int pos = (jj & 3) | (((p >> 1) & 1) << 2) | (((G & 1) ^ cmp) << 3) | ((jj >> 2) << 4);
      ta2[t] = (hf * 24 + G * 2) * 1024 + pos * 16 + 8 * (p & 1);
    }
#pragma unroll
    for (int be = 0; be < 3; ++be) {
      if (be == 0) vmw<12>(); else if (be == 1) vmw<6>(); else vmw<3>();
      barrier();
      if (be == 1) {
        if (w == 0) wait_flags(ws, WS_AFLAG + j * 768, 64, tag, l, 6);
        if (w == 1) wait_flags(ws, WS_AFLAG + j * 768 + 512, 32, tag, l, 7);
      }
#pragma unroll
      for (int i = 0; i < 3; ++i) {
#pragma unroll
        for (int nu = 0; nu < 4; ++nu) {
          const u32 o = be * 49152 + i * 8192 + (nu >> 1) * 1024 + (nu & 1) * 512;
          const v2u lo = ldstr(sm, ta2[0] + o), hi = ldstr(sm, ta2[1] + o);
          const v4u Bf = {lo[0], lo[1], hi[0], hi[1]};
#pragma unroll
          for (int dt = 0; dt < 3; ++dt) o2[dt][nu] = mfma16(Cf[3 * be + i][dt], Bf, o2[dt][nu]);
        }
      }
      if (be == 1) {
        barrier();
#pragma unroll
        for (int i = 0; i < 3; ++i) {
          const int pc = w * 3 + i;
          if (pc < 12) dma_sc1((u32)((96 * j + 8 * pc + (l >> 3)) * 4096 + c * 128 + (l & 7) * 16), ws + WS_AT, sb + B_AT + pc * 1024);
          else { const int e = (pc - 12) * 64 + l, row = e / 12, cc = e % 12;
                 dma((u32)((c * 64 + row) * 1152 + j * 192 + cc * 16), P.u, sb + B_XT + (pc - 12) * 1024); }
        }
      }
    }
  }
  stamp(ws, b, w, l, 10);
  if (w == 0) wait_flags(ws, WS_UFLAG + 2 * c * 8, 2, tag, l, 8);
  vm0();
  barrier();
  if (tid < 64) stlf(sm, B_GV + tid * 4, ldgf(ws + WS_G + (c * 64 + tid) * 4));
  barrier();
  {
#pragma unroll
    for (int nu = 0; nu < 4; ++nu) {
      const float gv = ldsf(sm, B_GV + (16 * nu + x) * 4), om = rbf(1.f - gv);
#pragma unroll
      for (int dt = 0; dt < 3; ++dt) {
        f4 v = o2[dt][nu] * (2.f * om);
        if (nu == kq) {
          const int dl = 48 * dh + 16 * dt + 4 * G;
          const int q = (l & 15) >> 2, p = l & 3;
          const v2u av = ldstr(sm, B_AT + (dl + q) * 128 + (16 * nu + 4 * p) * 2);
          const v2u xv = lds64(sm, B_XT + (16 * nu + x) * 192 + dl * 2);
          const v2u dv = *(const GAS v2u*)((const char*)P.Dsk2 + (96 * j + dl) * 2);
          v[0] += rbf(gv * bflo(av[0])) + om * rbf(bflo(xv[0]) * bflo(dv[0]));
          v[1] += rbf(gv * bfhi(av[0])) + om * rbf(bfhi(xv[0]) * bfhi(dv[0]));
          v[2] += rbf(gv * bflo(av[1])) + om * rbf(bflo(xv[1]) * bflo(dv[1]));
          v[3] += rbf(gv * bfhi(av[1])) + om * rbf(bfhi(xv[1]) * bfhi(dv[1]));
        }
        o2[dt][nu] = v;
      }
    }
  }
  barrier();
#pragma unroll
  for (int nu = 0; nu < 4; ++nu)
#pragma unroll
    for (int dt = 0; dt < 3; ++dt)
      stl128(sm, B_RED + kq * RED_Q + (16 * nu + x) * RED_ROW + (48 * dh + 16 * dt + 4 * G) * 4, __builtin_bit_cast(v4u, o2[dt][nu]));
  barrier();
#pragma unroll
  for (int i = 0; i < 3; ++i) {
    const int e = tid + 512 * i, t = e / 24, d4 = e % 24;
    const u32 o = B_RED + t * RED_ROW + d4 * 16;
    const f4 v = __builtin_bit_cast(f4, lds128(sm, o)) + __builtin_bit_cast(f4, lds128(sm, o + RED_Q)) +
                 __builtin_bit_cast(f4, lds128(sm, o + 2 * RED_Q)) + __builtin_bit_cast(f4, lds128(sm, o + 3 * RED_Q));
    *(GAS f4*)(P.out + (size_t)(c * 64 + t) * D_ + j * 96 + d4 * 4) = v;
  }
  stamp(ws, b, w, l, 11);
  if (b == 0 && w == 0) {
#pragma unroll 1
    for (int i = 0; i < 4; ++i) wait_flags(ws, WS_TOK + i * 512, 64, tag, l, 9);
    if (l == 0) stg64(ws + WS_E, tag);
  }
}

__global__ void __launch_bounds__(512, 2) gated_mix_kernel(Params P) {
  extern __shared__ __attribute__((aligned(16))) char smem_raw[];
  LAS char* sm = (LAS char*)smem_raw;
  const int tid = threadIdx.x, l = tid & 63, w = rfl(tid >> 6);
  const int b = blockIdx.x, xcd = b & 7, r = b >> 3;
  stamp(P.ws, b, w, l, 0);
  if (r < 24) b_wg(P, sm, xcd * 4 + r / 6, r % 6, b, w, l, tid);
  else spare_wg(P, sm, xcd * 8 + (r - 24), b, w, l, tid);
}

extern "C" void kernel_launch(void* const* d_in, const int* in_sizes, int n_in, void* d_out, int out_size, void* d_ws, size_t ws_size,
                              hipStream_t stream) {
  Params P;
  P.u = (const u16*)d_in[0]; P.logA = (const u16*)d_in[1]; P.Aim = (const u16*)d_in[2]; P.Cre = (const u16*)d_in[3]; P.Cim = (const u16*)d_in[4];
  P.logdt = (const u16*)d_in[5]; P.Dsk = (const u16*)d_in[6];
  P.Lre = (const u16*)d_in[8]; P.Lim = (const u16*)d_in[9]; P.Bre = (const u16*)d_in[10]; P.Bim = (const u16*)d_in[11];
  P.C2re = (const u16*)d_in[12]; P.C2im = (const u16*)d_in[13]; P.logdt2 = (const u16*)d_in[14]; P.Dsk2 = (const u16*)d_in[15]; P.gw = (const u16*)d_in[16];
  P.out = (float*)d_out; P.ws = (char*)d_ws;
  hipFuncSetAttribute((const void*)gated_mix_kernel, hipFuncAttributeMaxDynamicSharedMemorySize, LDS_BYTES);
  hipLaunchKernelGGL(gated_mix_kernel, dim3(256), dim3(512), LDS_BYTES, stream, P);
}
```

```cpp
#include <hip/hip_runtime.h>
#include <stdint.h>

typedef unsigned short u16;
typedef unsigned u32;
typedef unsigned long long u64;
typedef float f2 __attribute__((ext_vector_type(2)));
typedef float f4 __attribute__((ext_vector_type(4)));
typedef u32 v2u __attribute__((ext_vector_type(2)));
typedef u32 v4u __attribute__((ext_vector_type(4)));
typedef __bf16 bf2 __attribute__((ext_vector_type(2)));
typedef __bf16 bf8 __attribute__((ext_vector_type(8)));
typedef short s4v __attribute__((ext_vector_type(4)));
#define GAS __attribute__((address_space(1)))
#define LAS __attribute__((address_space(3)))
#define DEVI __device__ __forceinline__
#define RLX_AGENT __ATOMIC_RELAXED, __HIP_MEMORY_SCOPE_AGENT

constexpr int L_ = 2048, D_ = 576, N_ = 128;
constexpr u32 WS_E = 0, WS_ERR = 64, WS_TOK = 4096;
constexpr u32 WS_UFLAG = 786432, WS_CFLAG = WS_UFLAG + 8192, WS_GFLAG = WS_CFLAG + 8192, WS_HFLAG = WS_GFLAG + 8192, WS_AFLAG = WS_HFLAG + 24576,
              WS_USUM = WS_AFLAG + 576 * 128, WS_CSUM = WS_USUM + 1024;
constexpr u32 WS_Z = 458752;
constexpr u32 WS_G = 65536, WS_S = 131072, WS_UT = 1u << 20, WS_AT = 4u << 20, WS_HT = 8u << 20, WS_CT = 16u << 20;
constexpr u32 WS_STAMP = 20u << 20;
constexpr u32 WS_BYTES = 24u << 20;
#define GPRIO 1
#define MAPV 2
#define CODEKB 48
constexpr int CODE_KB = CODEKB;
constexpr u32 LDS_BYTES = 163840;
constexpr u32 KC_STRIDE = 4160, KC_BYTES = 4 * KC_STRIDE;
constexpr u32 UB_BYTES = 4608;
constexpr u32 KD_BYTES = 4128;
constexpr u32 PR_BYTES = 2048;
constexpr u32 LT_BYTES = 1728;
constexpr u32 B_RING = 0, B_KC = 98304, B_UB = B_KC + 2 * KC_BYTES, B_KD = B_UB + 2 * UB_BYTES, B_PR = B_KD + 2 * KD_BYTES,
              B_LT = B_PR + 2 * PR_BYTES, B_MISC = B_LT + 4 * LT_BYTES;
static_assert(B_MISC + 128 <= LDS_BYTES, "lds");
constexpr u32 KT_BYTES = 24 * 288;
constexpr u32 B_XK = 0, B_KT = 16384, B_XY = 45056;
static_assert(B_KT + 4 * KT_BYTES <= B_XY && B_XY + 16384 <= 65536, "ring slots 0 / 1");
constexpr u32 B_AT = 147456, B_RED = 0, RED_ROW = 400, RED_Q = 64 * RED_ROW;
static_assert(B_RED + 4 * RED_Q <= B_AT && B_AT + 12288 <= B_MISC, "lds");
constexpr u32 S_X = 0, S_ST = 36864, S_KC = S_ST + 18432, S_UB = S_KC + 3 * KC_BYTES, S_KD = S_UB + 3 * UB_BYTES, S_PR = S_KD + 3 * KD_BYTES,
              S_XY = S_PR + 3 * PR_BYTES, S_MISC = S_XY + 6 * 4096, S_GW = S_MISC + 128;
static_assert(S_GW + 1152 <= LDS_BYTES, "lds");

struct Params {
  const u16 *u, *logA, *Aim, *Cre, *Cim, *logdt, *Dsk, *Lre, *Lim, *Bre, *Bim, *C2re, *C2im, *logdt2, *Dsk2, *gw;
  float* out;
  char* ws;
};

struct Tbl { u16 v[2064]; };
constexpr Tbl make_tbl() {
  Tbl t{};
  for (int x = 0; x < 2064; ++x) {
    int l = 2047 - x;
    int dw = 1025;
    if (l >= 0) {
      int e = 0;
      while ((l >> e) >= 256) ++e;
      int pos = l;
      if (e > 0) {
        int m = l >> e, rem = l & ((1 << e) - 1), half = 1 << (e - 1);
        if (rem > half || (rem == half && (m & 1))) ++m;
        pos = m << e;
      }
      if (pos == 2048) dw = 1024;
      else {
        int seg = pos < 256 ? 0 : pos < 512 ? 1 : pos < 1024 ? 2 : 3;
        dw = seg * 256 + (pos >> seg);
      }
    }
    t.v[x] = (u16)(dw * 4);
  }
  return t;
}
__device__ const Tbl g_tbl = make_tbl();

DEVI float bf2f(u32 v) { return __uint_as_float(v << 16); }
DEVI float bflo(u32 v) { return __uint_as_float(v << 16); }
DEVI float bfhi(u32 v) { return __uint_as_float(v & 0xffff0000u); }
DEVI u32 pk2(float a, float b) { bf2 t = {(__bf16)a, (__bf16)b}; return __builtin_bit_cast(u32, t); }
DEVI float rbf(float a) { return bflo(pk2(a, a)); }
DEVI f4 mfma16(v4u a, v4u b, f4 c) { return __builtin_amdgcn_mfma_f32_16x16x32_bf16(__builtin_bit_cast(bf8, a), __builtin_bit_cast(bf8, b), c, 0, 0, 0); }
DEVI f4 mfma16h(v2u a, v2u b, f4 c) { return __builtin_amdgcn_mfma_f32_16x16x16bf16_1k(__builtin_bit_cast(s4v, a), __builtin_bit_cast(s4v, b), c, 0, 0, 0); }
DEVI int rfl(int v) { return __builtin_amdgcn_readfirstlane(v); }
DEVI void barrier() { asm volatile("s_waitcnt lgkmcnt(0)\n\ts_barrier" ::: "memory"); }
DEVI void vm0() { asm volatile("s_waitcnt vmcnt(0)" ::: "memory"); }
template <int N> DEVI void vmw() { asm volatile("s_waitcnt vmcnt(%0)" ::"n"(N) : "memory"); }
DEVI void dma(u32 voff, const void* sbase, u32 dst) {
  u32 keep;
  asm volatile("s_mov_b32 %0, m0\n\ts_mov_b32 m0, %3\n\ts_nop 0\n\tglobal_load_lds_dwordx4 %1, %2\n\ts_mov_b32 m0, %0"
               : "=&s"(keep) : "v"(voff), "s"(sbase), "s"(dst) : "memory");
}
DEVI void dma_sc1(u32 voff, const void* sbase, u32 dst) {
  u32 keep;
  asm volatile("s_mov_b32 %0, m0\n\ts_mov_b32 m0, %3\n\ts_nop 0\n\tglobal_load_lds_dwordx4 %1, %2 sc1\n\ts_mov_b32 m0, %0"
               : "=&s"(keep) : "v"(voff), "s"(sbase), "s"(dst) : "memory");
}
DEVI v4u lds128(LAS char* sm, u32 off) { return *(LAS v4u*)(sm + off); }
DEVI v2u lds64(LAS char* sm, u32 off) { return *(LAS v2u*)(sm + off); }
DEVI float ldsf(LAS char* sm, u32 off) { return *(LAS float*)(sm + off); }
DEVI void stl128(LAS char* sm, u32 off, v4u v) { *(LAS v4u*)(sm + off) = v; }
DEVI void stl64(LAS char* sm, u32 off, v2u v) { *(LAS v2u*)(sm + off) = v; }
DEVI void stlf(LAS char* sm, u32 off, float v) { *(LAS float*)(sm + off) = v; }
DEVI v2u ldstr(LAS char* sm, u32 off) { return __builtin_bit_cast(v2u, __builtin_amdgcn_ds_read_tr16_b64_v4i16((LAS s4v*)(sm + off))); }
DEVI u64 ldg64(const char* p) { return __hip_atomic_load((GAS u64*)p, RLX_AGENT); }
DEVI void stg64(char* p, u64 v) { __hip_atomic_store((GAS u64*)p, v, RLX_AGENT); }
DEVI float ldgf(const char* p) { return __uint_as_float(__hip_atomic_load((GAS u32*)p, RLX_AGENT)); }
DEVI void stgf(char* p, float v) { __hip_atomic_store((GAS u32*)p, __float_as_uint(v), RLX_AGENT); }
DEVI u32 ldu16(const u16* p) { return *(const GAS u16*)p; }

DEVI f2 csq(f2 s) {
  f2 p, r;
  asm("v_pk_mul_f32 %0, %1, %1 op_sel_hi:[0,1]" : "=v"(p) : "v"(s));
  asm("v_pk_fma_f32 %0, %1, %1, %2 op_sel:[1,1,0] op_sel_hi:[0,1,1] neg_lo:[1,0,0]" : "=v"(r) : "v"(s), "v"(p));
  return r;
}
DEVI f2 cmul(f2 a, f2 b) {
  f2 p, r;
  asm("v_pk_mul_f32 %0, %1, %2 op_sel_hi:[0,1]" : "=v"(p) : "v"(a), "v"(b));
  asm("v_pk_fma_f32 %0, %1, %2, %3 op_sel:[1,1,0] op_sel_hi:[1,0,1] neg_lo:[1,0,0]" : "=v"(r) : "v"(a), "v"(b), "v"(p));
  return r;
}
DEVI f2 cmulc(f2 a, f2 b) {
  f2 p, r;
  asm("v_pk_mul_f32 %0, %1, %2 op_sel_hi:[0,1] neg_hi:[0,1]" : "=v"(p) : "v"(a), "v"(b));
  asm("v_pk_fma_f32 %0, %1, %2, %3 op_sel:[1,1,0] op_sel_hi:[1,0,1] neg_lo:[1,0,0] neg_hi:[1,0,0]" : "=v"(r) : "v"(a), "v"(b), "v"(p));
  return r;
}
DEVI void cmul2(f2& a0, f2 b0, f2& a1, f2 b1) {
  f2 p0, p1, r0, r1;
  asm("v_pk_mul_f32 %0, %4, %5 op_sel_hi:[0,1]\n\tv_pk_mul_f32 %1, %6, %7 op_sel_hi:[0,1]\n\t"
      "v_pk_fma_f32 %2, %4, %5, %0 op_sel:[1,1,0] op_sel_hi:[1,0,1] neg_lo:[1,0,0]\n\t"
      "v_pk_fma_f32 %3, %6, %7, %1 op_sel:[1,1,0] op_sel_hi:[1,0,1] neg_lo:[1,0,0]"
      : "=&v"(p0), "=&v"(p1), "=&v"(r0), "=&v"(r1) : "v"(a0), "v"(b0), "v"(a1), "v"(b1));
  a0 = r0; a1 = r1;
}
DEVI void csq2(f2& a0, f2& a1) {
  f2 p0, p1, r0, r1;
  asm("v_pk_mul_f32 %0, %4, %4 op_sel_hi:[0,1]\n\tv_pk_mul_f32 %1, %5, %5 op_sel_hi:[0,1]\n\t"
      "v_pk_fma_f32 %2, %4, %4, %0 op_sel:[1,1,0] op_sel_hi:[0,1,1] neg_lo:[1,0,0]\n\t"
      "v_pk_fma_f32 %3, %5, %5, %1 op_sel:[1,1,0] op_sel_hi:[0,1,1] neg_lo:[1,0,0]"
      : "=&v"(p0), "=&v"(p1), "=&v"(r0), "=&v"(r1) : "v"(a0), "v"(a1));
  a0 = r0; a1 = r1;
}
DEVI float swap8(float h) { return __int_as_float(__builtin_amdgcn_update_dpp(0, __float_as_int(h), 0x128, 0xf, 0xf, true)); }
DEVI float lmul_add(float a, float s, float h, float g) { return __builtin_fmaf(s, swap8(h), __builtin_fmaf(a, h, g)); }

constexpr u32 SPIN_MAX = 1u << 18;
DEVI void spin_fail(char* ws, u32 code) { __hip_atomic_store((GAS u32*)(ws + WS_ERR), code, RLX_AGENT); }
template <int SLEEP> DEVI void wait_lane_flags(char* ws, u32 off, u64 tag, int l, u32 code) {
  for (u32 s = 0;; ++s) {
    const u64 v = ldg64(ws + off);
    if (__all(v == tag)) break;
    if (s > SPIN_MAX) { if (l == 0) spin_fail(ws, code); break; }
    __builtin_amdgcn_s_sleep(SLEEP);
  }
}
DEVI bool test_lane_flags(char* ws, u32 off, u64 tag) { return __all(ldg64(ws + off) == tag); }
#define STAMP_DECL
#define stamp(ws_, b_, w_, l_, i_) do {} while (0)
#define stampc(ws_, b_, w_, l_, i_) do {} while (0)
#define STAMP_FLUSH(ws_, b_, w_, l_) do {} while (0)
#define STV_PARAM
#define STV_ARG
template <int N> struct IC { static constexpr int value = N; };
template <int N, int I = 0, class F> DEVI void static_for(F&& f) { if constexpr (I < N) { f(IC<I>{}); static_for<N, I + 1>(f); } }
DEVI float exp_acc(float x) {
  const float t = x * 1.44269502162933349609375f;
  const float e = __builtin_fmaf(x, 1.44269502162933349609375f, -t) + x * 1.925963033500011079e-8f;
  return __builtin_amdgcn_exp2f(t) * __builtin_fmaf(e, 0.6931471805599453f, 1.f);
}
DEVI f2 cis(float x) {
  const float r = x * 0.15915494309189535f, k = __builtin_rintf(r);
  const float f = __builtin_fmaf(x, 0.15915494309189535f, -k) + x * 6.4206382e-9f;
  return (f2){__builtin_amdgcn_cosf(f), __builtin_amdgcn_sinf(f)};
}
DEVI u32 ald_u16(u32 voff, const void* sbase) { u32 r; asm volatile("global_load_ushort %0, %1, %2" : "=v"(r) : "v"(voff), "s"(sbase) : "memory"); return r; }
DEVI v4u ald_128(u32 voff, const void* sbase) { v4u r; asm volatile("global_load_dwordx4 %0, %1, %2" : "=v"(r) : "v"(voff), "s"(sbase) : "memory"); return r; }
DEVI v2u ald_64(u32 voff, const void* sbase) { v2u r; asm volatile("global_load_dwordx2 %0, %1, %2" : "=v"(r) : "v"(voff), "s"(sbase) : "memory"); return r; }
DEVI u32 ald_32_sc1(u32 voff, const void* sbase) { u32 r; asm volatile("global_load_dword %0, %1, %2 sc1" : "=v"(r) : "v"(voff), "s"(sbase) : "memory"); return r; }
DEVI v2u ald_64_sc1(u32 voff, const void* sbase) { v2u r; asm volatile("global_load_dwordx2 %0, %1, %2 sc1" : "=v"(r) : "v"(voff), "s"(sbase) : "memory"); return r; }
#define PIN(x) asm volatile("" : "+v"(x))
#define SB() __builtin_amdgcn_sched_barrier(0)
DEVI void dma2(u32 v0, u32 v1, const char* base, u32 m0v) {
  asm volatile("s_mov_b32 m0, %3\n\ts_nop 0\n\tglobal_load_lds_dwordx4 %0, %2\n\tglobal_load_lds_dwordx4 %1, %2 offset:1024"
               :: "v"(v0), "v"(v1), "s"(base), "s"(m0v) : "memory");
}
DEVI void dma6(u32 v0, u32 v1, u32 v2, u32 v3, u32 v4, u32 v5, const char* ba, const char* bb, u32 m0v) {
  asm volatile("s_mov_b32 m0, %8\n\ts_nop 0\n\tglobal_load_lds_dwordx4 %0, %6\n\tglobal_load_lds_dwordx4 %1, %7 offset:1024\n\t"
               "global_load_lds_dwordx4 %2, %6 offset:2048\n\tglobal_load_lds_dwordx4 %3, %7 offset:3072\n\t"
               "s_add_u32 m0, m0, 0x1000\n\ts_nop 0\n\tglobal_load_lds_dwordx4 %4, %6\n\tglobal_load_lds_dwordx4 %5, %7 offset:1024"
               :: "v"(v0), "v"(v1), "v"(v2), "v"(v3), "v"(v4), "v"(v5), "s"(ba), "s"(bb), "s"(m0v) : "memory", "scc");
}
DEVI void dma6_lin(u32 v, const char* base, u32 m0v) {
  asm volatile("s_mov_b32 m0, %2\n\ts_nop 0\n\tglobal_load_lds_dwordx4 %0, %1 sc1\n\tglobal_load_lds_dwordx4 %0, %1 offset:1024 sc1\n\t"
               "global_load_lds_dwordx4 %0, %1 offset:2048 sc1\n\tglobal_load_lds_dwordx4 %0, %1 offset:3072 sc1\n\t"
               "s_add_u32 m0, m0, 0x1000\n\ts_nop 0\n\tglobal_load_lds_dwordx4 %3, %1 sc1\n\tglobal_load_lds_dwordx4 %3, %1 offset:1024 sc1"
               :: "v"(v), "s"(base), "s"(m0v), "v"(v + 4096) : "memory", "scc");
}
template <int OFF> DEVI v2u ald_64_sc1o(u32 voff, const void* sbase) { v2u r; asm volatile("global_load_dwordx2 %0, %1, %2 offset:%3 sc1" : "=v"(r) : "v"(voff), "s"(sbase), "n"(OFF) : "memory"); return r; }
#define DPP8 " row_ror:8 row_mask:0xf bank_mask:0xf"
DEVI void scan_keep(float& h0, float& h1, float& h2, float g0, float g1, float g2, const float (&a)[3], const float (&s)[3]) {
  float t0, t1, t2;
  asm("v_fma_f32 %0, %9, %3, %6\n\tv_fma_f32 %1, %10, %4, %7\n\tv_fma_f32 %2, %11, %5, %8\n\t"
      "v_fmac_f32_dpp %0, %3, %12" DPP8 "\n\tv_fmac_f32_dpp %1, %4, %13" DPP8 "\n\tv_fmac_f32_dpp %2, %5, %14" DPP8
      : "=&v"(t0), "=&v"(t1), "=&v"(t2)
      : "v"(h0), "v"(h1), "v"(h2), "v"(g0), "v"(g1), "v"(g2), "v"(a[0]), "v"(a[1]), "v"(a[2]), "v"(s[0]), "v"(s[1]), "v"(s[2]));
  h0 = t0; h1 = t1; h2 = t2;
}
DEVI void scan_out(float& h0, float& h1, float& h2, float g0, float g1, float g2, const float (&a)[3], const float (&s)[3],
                   const float (&ca)[3], const float (&cs)[3], float& o0, float& o1, float& o2) {
  scan_keep(h0, h1, h2, g0, g1, g2, a, s);
  asm("v_mul_f32 %0, %6, %3\n\tv_mul_f32 %1, %7, %4\n\tv_mul_f32 %2, %8, %5\n\t"
      "v_fmac_f32_dpp %0, %3, %9" DPP8 "\n\tv_fmac_f32_dpp %1, %4, %10" DPP8 "\n\tv_fmac_f32_dpp %2, %5, %11" DPP8
      : "=&v"(o0), "=&v"(o1), "=&v"(o2)
      : "v"(h0), "v"(h1), "v"(h2), "v"(ca[0]), "v"(ca[1]), "v"(ca[2]), "v"(cs[0]), "v"(cs[1]), "v"(cs[2]));
}
DEVI void pair_sync(LAS char* sm, u32 off, u32 target, int l) {
  asm volatile("s_waitcnt lgkmcnt(0)" ::: "memory");
  if (l == 0) __hip_atomic_fetch_add((LAS u32*)(sm + off), 1u, __ATOMIC_RELAXED, __HIP_MEMORY_SCOPE_WORKGROUP);
  for (u32 s = 0; s < (1u << 22); ++s) {
    const u32 v = (u32)rfl((int)__hip_atomic_load((LAS u32*)(sm + off), __ATOMIC_RELAXED, __HIP_MEMORY_SCOPE_WORKGROUP));
    if (v >= target) break;
  }
  asm volatile("" ::: "memory");
}

DEVI void arrive(LAS char* sm, u32 off, int l) {
  asm volatile("s_waitcnt lgkmcnt(0)" ::: "memory");
  if (l == 0) __hip_atomic_fetch_add((LAS u32*)(sm + off), 1u, __ATOMIC_RELAXED, __HIP_MEMORY_SCOPE_WORKGROUP);
}
template <int OFF> DEVI v4u ald_128_sc1o(u32 voff, const void* sbase) { v4u r; asm volatile("global_load_dwordx4 %0, %1, %2 offset:%3 sc1" : "=v"(r) : "v"(voff), "s"(sbase), "n"(OFF) : "memory"); return r; }
DEVI void lds_set(LAS char* sm, u32 off, int l) { if (l == 0) __hip_atomic_store((LAS u32*)(sm + off), 1u, __ATOMIC_RELAXED, __HIP_MEMORY_SCOPE_WORKGROUP); }
DEVI void lds_wait(LAS char* sm, u32 off) {
  for (u32 s = 0; s < (1u << 22); ++s) {
    if (rfl((int)__hip_atomic_load((LAS u32*)(sm + off), __ATOMIC_RELAXED, __HIP_MEMORY_SCOPE_WORKGROUP))) break;
    __builtin_amdgcn_s_sleep(1);
  }
  asm volatile("" ::: "memory");
}
DEVI void lds_wait_count(LAS char* sm, u32 off, u32 target) {
  for (u32 s = 0; s < (1u << 22); ++s) {
    if ((u32)rfl((int)__hip_atomic_load((LAS u32*)(sm + off), __ATOMIC_RELAXED, __HIP_MEMORY_SCOPE_WORKGROUP)) >= target) break;
    __builtin_amdgcn_s_sleep(1);
  }
  asm volatile("" ::: "memory");
}
DEVI float wave_sum(float v) {
  v += __int_as_float(__builtin_amdgcn_update_dpp(0, __float_as_int(v), 0xB1, 0xf, 0xf, true));
  v += __int_as_float(__builtin_amdgcn_update_dpp(0, __float_as_int(v), 0x4E, 0xf, 0xf, true));
  v += __int_as_float(__builtin_amdgcn_update_dpp(0, __float_as_int(v), 0x141, 0xf, 0xf, true));
  v += __int_as_float(__builtin_amdgcn_update_dpp(0, __float_as_int(v), 0x140, 0xf, 0xf, true));
  auto r1 = __builtin_amdgcn_permlane16_swap(__float_as_uint(v), __float_as_uint(v), false, false);
  v = __uint_as_float(r1[0]) + __uint_as_float(r1[1]);
  auto r2 = __builtin_amdgcn_permlane32_swap(__float_as_uint(v), __float_as_uint(v), false, false);
  return __uint_as_float(r2[0]) + __uint_as_float(r2[1]);
}
struct ARaw { u32 la, ai, cr, ci, ldt, dsk; v4u ta[2]; v2u tb[2]; };
DEVI void a_load_modes(const Params& P, int ch, int hr, int l, ARaw& R) {
  const u32 o = (u32)(ch * N_ + 64 * hr + l) * 2;
  R.la = ald_u16(o, P.logA); R.ai = ald_u16(o, P.Aim); R.cr = ald_u16(o, P.Cre); R.ci = ald_u16(o, P.Cim);
  R.ldt = ald_u16((u32)ch * 2, P.logdt);
}
DEVI void a_load_tab(const Params& P, int ch, int hr, int l, ARaw& R) {
  R.dsk = ald_u16((u32)ch * 2, P.Dsk);
#pragma unroll
  for (int i = 0; i < 2; ++i) { const u32 q = (u32)(128 * hr + 64 * i + l) * 16; R.ta[i] = ald_128(q, g_tbl.v); R.tb[i] = ald_64(q + 16, g_tbl.v); }
}
DEVI void a_pin_modes(ARaw& R) { PIN(R.la); PIN(R.ai); PIN(R.cr); PIN(R.ci); PIN(R.ldt); }
DEVI void a_pin_tab(ARaw& R) { PIN(R.dsk); PIN(R.ta[0]); PIN(R.ta[1]); PIN(R.tb[0]); PIN(R.tb[1]); }
constexpr int vrow(int s, int b) {
  if (s == 0) return b;
  return b < 8 ? vrow(s - 1, 2 * b) : vrow(s - 1, 2 * (b - 8) + 1);
}
constexpr u64 vrow_lut(int s) { u64 v = 0; for (int b = 0; b < 16; ++b) v |= (u64)vrow(s, b) << (4 * b); return v; }
template <class Hook>
DEVI float kgen_chain(LAS char* sm, u32 tb, int l, const ARaw& R, f4 (&kt)[4], Hook late) {
  const float dt = rbf(exp_acc(bf2f(R.ldt)));
  const float are = -rbf(exp_acc(bf2f(R.la)));
  const float ar2 = (are * dt) * 1.4426950408889634f, aiv = __builtin_amdgcn_fractf((bf2f(R.ai) * dt) * 0.15915494309189535f);
  const f2 C = {2.f * (bf2f(R.cr) * dt), 2.f * (bf2f(R.ci) * dt)};
  const float m1 = __builtin_amdgcn_exp2f(ar2), m16 = __builtin_amdgcn_exp2f(16.f * ar2);
  const f2 z = {m1 * __builtin_amdgcn_cosf(aiv), m1 * __builtin_amdgcn_sinf(aiv)};
  const f2 z16 = {m16 * __builtin_amdgcn_cosf(16.f * aiv), m16 * __builtin_amdgcn_sinf(16.f * aiv)};
  const u32 wv = tb + l * 4, ww = wv + 16 * 288;
  const u32 rw = tb + (16 + (l & 7)) * 288 + (l >> 4) * 16;
  auto st32 = [&](u32 a, u32 v) { *(LAS u32*)(sm + a) = v; };
  auto pkv = [&](f2 v) { return pk2(v.x, v.y); };
  auto pkw = [&](f2 v) { return pk2(v.x, -v.y); };
  v4u Vf[4], Wf[4];
  auto ldV = [&](int s) {
    const u32 rv = tb + (u32)((vrow_lut(s) >> (4 * (l & 15))) & 15) * 288 + (l >> 4) * 16;
#pragma unroll
    for (int k = 0; k < 4; ++k) Vf[k] = lds128(sm, rv + 64 * k);
  };
  auto ldW = [&]() {
#pragma unroll
    for (int k = 0; k < 4; ++k) Wf[k] = lds128(sm, rw + 64 * k);
  };
  auto tile = [&]() -> f4 {
    f4 d = {0.f, 0.f, 0.f, 0.f}, d2 = {0.f, 0.f, 0.f, 0.f};
    d = mfma16(Vf[0], Wf[0], d); d2 = mfma16(Vf[1], Wf[1], d2);
    d = mfma16(Vf[2], Wf[2], d); d2 = mfma16(Vf[3], Wf[3], d2);
    return d + d2;
  };
  f2 z2 = z, zq = z16;
  csq2(z2, zq);
  f2 vf[8];
  f2 e = z2, o = z;
  f2 we = C, wo = cmul(C, z16);
  st32(wv, 0x3f80u); st32(wv + 288, pkv(z));
#pragma unroll
  for (int k = 0; k < 4; ++k) {
    st32(ww + 2 * k * 288, pkw(we)); st32(ww + (2 * k + 1) * 288, pkw(wo));
    cmul2(we, zq, wo, zq);
    f2 en = e;
    cmul2(o, z2, en, z2);
    st32(wv + (2 * k + 2) * 288, pkv(e)); st32(wv + (2 * k + 3) * 288, pkv(o));
    if (k == 3) { vf[0] = e; vf[1] = o; }
    e = en;
  }
#pragma unroll
  for (int k = 5; k < 8; ++k) {
    f2 en = e;
    cmul2(o, z2, en, z2);
    st32(wv + 2 * k * 288, pkv(e)); st32(wv + (2 * k + 1) * 288, pkv(o));
    vf[2 * k - 8] = e; vf[2 * k - 7] = o;
    e = en;
  }
  ldV(0); ldW();
  SB();
#pragma unroll
  for (int k = 0; k < 4; ++k) {
    st32(ww + 2 * k * 288, pkw(we)); st32(ww + (2 * k + 1) * 288, pkw(wo));
    if (k < 3) cmul2(we, zq, wo, zq);
  }
  SB();
  const f4 d0a = tile();
  ldW();
  SB();
  auto rows = [&](auto s_) {
    constexpr int s = decltype(s_)::value;
    const f2 zn = csq(zq);
    wo = we;
    cmul2(we, zq, wo, zn);
    zq = zn;
#pragma unroll
    for (int k = 0; k < 4; ++k) {
      csq2(vf[2 * k], vf[2 * k + 1]);
      st32(ww + 2 * k * 288, pkw(we)); st32(ww + (2 * k + 1) * 288, pkw(wo));
      if (k < 3) cmul2(we, zq, wo, zq);
      st32(wv + vrow(s, 8 + 2 * k) * 288, pkv(vf[2 * k])); st32(wv + vrow(s, 9 + 2 * k) * 288, pkv(vf[2 * k + 1]));
    }
  };
  rows(IC<1>{});
  SB();
  { const f4 d0b = tile();
#pragma unroll
    for (int r = 0; r < 4; ++r) kt[0][r] = (l & 8) ? d0b[r] : d0a[r]; }
  ldV(1); ldW(); SB(); rows(IC<2>{}); SB(); kt[1] = tile();
  late();
  ldV(2); ldW(); SB(); rows(IC<3>{}); SB(); kt[2] = tile();
  ldV(3); ldW();
  const float k2048 = wave_sum(cmul(we, zq).x);
  kt[3] = tile();
  return k2048;
}
DEVI void expand_chunk(LAS char* sm, u32 kd, u32 kc, int q, v4u ta, v2u tb) {
  float e[11];
#pragma unroll
  for (int i = 0; i < 11; ++i) {
    const u32 wv = i < 8 ? ta[i >> 1] : tb[(i - 8) >> 1];
    e[i] = ldsf(sm, kd + ((i & 1) ? (wv >> 16) : (wv & 0xffffu)));
  }
  u32 Pk[5], Qk[5];
#pragma unroll
  for (int k = 0; k < 5; ++k) { Pk[k] = pk2(e[2 * k], e[2 * k + 1]); Qk[k] = pk2(e[2 * k + 1], e[2 * k + 2]); }
  stl128(sm, kc + 3 * KC_STRIDE + q * 16, (v4u){Pk[0], Pk[1], Pk[2], Pk[3]});
  stl128(sm, kc + 2 * KC_STRIDE + q * 16, (v4u){Qk[0], Qk[1], Qk[2], Qk[3]});
  stl128(sm, kc + 1 * KC_STRIDE + q * 16, (v4u){Pk[1], Pk[2], Pk[3], Pk[4]});
  stl128(sm, kc + 0 * KC_STRIDE + q * 16, (v4u){Qk[1], Qk[2], Qk[3], Qk[4]});
}
DEVI void wait_summary(char* ws, bool agg, u32 flags, int nflags, u32 sum, u64 tag, int l, u32 code) {
  if (agg) { wait_lane_flags<6>(ws, flags + (l < nflags ? l : 0) * 128, tag, l, code); if (l == 0) stg64(ws + sum, tag); }
  else wait_lane_flags<6>(ws, sum, tag, l, code);
}
DEVI void fetch_u(char* ws, u32 sb, int ch, int hr, int l, u32 ub) {
#pragma unroll
  for (int i = 0; i < 2; ++i) dma_sc1((u32)((2 * hr + i) * 1024 + l * 16), ws + WS_UT + (size_t)ch * 4096, sb + ub + 512 + (2 * hr + i) * 1024);
}
template <bool EXTRA, class Hook1, class Hook2>
DEVI void a_finish(const Params& P, LAS char* sm, u32 sb, const ARaw& R, const f4 (&kt)[4], float k2048, int ch, int hr, int w, int l, u64 tag,
                   u32 xown, u32 xoth, u32 k2a, u32 xy, u32 kd, u32 kc, u32 ub, u32 cnt, __amdgpu_buffer_rsrc_t rs, int b, Hook1 before_conv, Hook2 after_stores STV_PARAM) {
  char* ws = P.ws;
  u32 cb = 0;
#pragma unroll
  for (int k = 0; k < 4; ++k) stl128(sm, xown + k * 1024 + l * 16, __builtin_bit_cast(v4u, kt[k]));
  if (l == 0) stlf(sm, k2a + 4 * hr, k2048);
  pair_sync(sm, cnt, cb + 2, l); cb += 2;
  stamp(ws, b, w, l, 3);
#pragma unroll
  for (int k = 0; k < 4; ++k) {
    const f4 o = __builtin_bit_cast(f4, lds128(sm, xoth + k * 1024 + l * 16));
    const f4 v = hr ? o + kt[k] : kt[k] + o;
    stl128(sm, kd + (k * 256 + 16 * (l & 15) + 4 * (l >> 4)) * 4, __builtin_bit_cast(v4u, v));
  }
  if (l == 0) { stlf(sm, kd + 4096, ldsf(sm, k2a) + ldsf(sm, k2a + 4)); stlf(sm, kd + 4100, 0.f); }
  if (EXTRA) { pair_sync(sm, cnt, cb + 2, l); cb += 2; }
  else asm volatile("s_waitcnt lgkmcnt(0)" ::: "memory");
  if (hr == 1 && l < 8) stl128(sm, kc + (l >> 1) * KC_STRIDE + 4096 + (l & 1) * 16, (v4u){0, 0, 0, 0});
#pragma unroll
  for (int i = 0; i < 2; ++i) expand_chunk(sm, kd, kc, 128 * hr + 64 * i + l, R.ta[i], R.tb[i]);
  stamp(ws, b, w, l, 4);
  before_conv();
  vm0();
  pair_sync(sm, cnt, cb + 2, l);
  stamp(ws, b, w, l, 5);
  f4 acc[8];
  {
    const int r = l & 15, g = l >> 4;
    u32 t0 = kc + (r & 3) * KC_STRIDE + 2 * (2028 - 4 * (r >> 2) + 8 * g) - 512 * 7 - 256 * hr;
    u32 u0 = ub + 2 * (256 - 16 + 16 * r + 8 * g) - 256 * hr;
#pragma unroll
    for (int i = 0; i < 8; ++i) acc[i] = (f4){0.f, 0.f, 0.f, 0.f};
    u32 t8 = t0 + 8;
    PIN(t8);
    v4u Ua[8], Ub[8], T[4];
    auto ldT = [&](int n, int back) -> v4u {
      const v2u a0 = lds64(sm, t0 + 512 * (7 - n) - back), a1 = lds64(sm, t8 + 512 * (7 - n) - back);
      return (v4u){a0[0], a0[1], a1[0], a1[1]};
    };
    auto resid = [&](v4u (&U)[8], v4u (&Un)[8]) {
      u0 -= 64;
      static_for<8>([&](auto n_) {
        constexpr int n = decltype(n_)::value;
        T[(n + 2) & 3] = n < 6 ? ldT(n + 2, 0) : ldT(n - 6, 64);
        Un[n] = lds128(sm, u0 + 512 * n);
#pragma unroll
        for (int i = n; i < 8; ++i) acc[i] = mfma16(T[n & 3], U[i - n], acc[i]);
        __builtin_amdgcn_sched_group_barrier(0x008, 1, 0); __builtin_amdgcn_sched_group_barrier(0x100, 1, 0);
        __builtin_amdgcn_sched_group_barrier(0x008, 1, 0); __builtin_amdgcn_sched_group_barrier(0x100, 1, 0);
        __builtin_amdgcn_sched_group_barrier(0x008, 1, 0); __builtin_amdgcn_sched_group_barrier(0x100, 1, 0);
        __builtin_amdgcn_sched_group_barrier(0x008, 8, 0);
        SB();
      });
      t0 -= 64; t8 -= 64;
    };
#pragma unroll
    for (int m = 0; m < 8; ++m) Ua[m] = lds128(sm, u0 + 512 * m);
    T[0] = ldT(0, 0); T[1] = ldT(1, 0);
#pragma unroll 1
    for (int it = 0; it < 2; ++it) { resid(Ua, Ub); resid(Ub, Ua); }
  }
  stamp(ws, b, w, l, 6);
  f4 y[4];
  if (hr) {
#pragma unroll
    for (int i = 0; i < 4; ++i) { stl128(sm, xy + (w * 4 + i) * 1024 + l * 16, __builtin_bit_cast(v4u, acc[i])); y[i] = acc[4 + i]; }
  } else {
#pragma unroll
    for (int i = 0; i < 4; ++i) { stl128(sm, xy + (w * 4 + i) * 1024 + l * 16, __builtin_bit_cast(v4u, acc[4 + i])); y[i] = acc[i]; }
  }
  pair_sync(sm, cnt, cb + 4, l);
  stamp(ws, b, w, l, 16);
  const float dsk = bf2f(R.dsk);
#pragma unroll
  for (int i = 0; i < 4; ++i) {
    y[i] += __builtin_bit_cast(f4, lds128(sm, xy + ((w ^ 1) * 4 + i) * 1024 + l * 16));
    const u32 o = ub + 512 + 2 * (256 * (4 * hr + i) + 16 * (l & 15) + 4 * (l >> 4));
    const v2u uu = lds64(sm, o);
    const float a0 = rbf(y[i][0]) + rbf(bflo(uu[0]) * dsk), a1 = rbf(y[i][1]) + rbf(bfhi(uu[0]) * dsk);
    const float a2 = rbf(y[i][2]) + rbf(bflo(uu[1]) * dsk), a3 = rbf(y[i][3]) + rbf(bfhi(uu[1]) * dsk);
    stl64(sm, o, (v2u){pk2(a0, a1), pk2(a2, a3)});
  }
  asm volatile("s_waitcnt lgkmcnt(0)" ::: "memory");
#pragma unroll
  for (int i = 0; i < 2; ++i) {
    const u32 e = 1024 * hr + 512 * i + 8 * l;
    __builtin_amdgcn_raw_buffer_store_b128(lds128(sm, ub + 512 + 2 * e), rs, WS_AT + (u32)ch * 4096u + 2 * e, 0, 16);
  }
  stamp(ws, b, w, l, 15);
  after_stores();
  if (l == 0) stg64(ws + WS_AFLAG + ch * 128 + hr * 8, tag);
  stamp(ws, b, w, l, 7);
}
DEVI void a_zero_pad(LAS char* sm, u32 ub, int hr, int l) { if (hr == 0 && l < 32) stl128(sm, ub + l * 16, (v4u){0, 0, 0, 0}); }

DEVI void spare_wg(const Params& P, LAS char* sm, int sidx, int b, int w, int l, int tid STV_PARAM) {
  char* ws = P.ws;
  const u32 sb = (u32)(size_t)sm;
  const __amdgpu_buffer_rsrc_t rs = __builtin_amdgcn_make_buffer_rsrc(ws, 0, WS_BYTES, 0x00020000);
  const int pr = w >> 1, hr = w & 1, ch = 384 + 3 * sidx + pr;
  ARaw R;
  v2u ev = ald_64_sc1(0, ws + WS_E);
  if (w < 6) {
    a_load_modes(P, ch, hr, l, R); a_load_tab(P, ch, hr, l, R);
    a_zero_pad(sm, S_UB + pr * UB_BYTES, hr, l);
  } else {
    const char* xs = (const char*)P.u + (size_t)sidx * 36864 + hr * 18432;
#pragma unroll 1
    for (int i = 0; i < 3; ++i) dma6_lin((u32)(l * 16), xs + i * 6144, sb + S_X + hr * 18432 + i * 6144);
#pragma unroll 1
    for (int i = 0; i < 11; ++i) {
      const int fl = hr + 2 * i, f = sidx * 21 + fl;
      if (fl < 21 && f < 1296) {
        const int dt = f % 3, ii = (f / 3) % 9, kq = (f / 27) & 3, dd = f / 108;
        const int row = l >> 2, cc = l & 3;
        dma((u32)((dd * 48 + dt * 16 + row) * 1152 + (288 * (kq & 1) + 32 * ii + 8 * (cc ^ ((row >> 1) & 3))) * 2), (kq >> 1) ? P.C2im : P.C2re,
            sb + S_XY + fl * 1024);
      }
    }
  }
  if (tid < 8) *(LAS u32*)(sm + S_MISC + 16 + tid * 4) = 0;
  barrier();
  if (w < 6) {
    vm0();
    a_pin_modes(R); a_pin_tab(R); PIN(ev);
    const u64 tag = (((u64)ev[1] << 32) | ev[0]) + 1;
    arrive(sm, S_MISC + 32, l);
    stamp(ws, b, w, l, 1);
    const u32 kc = S_KC + pr * KC_BYTES, cnt = S_MISC + 16 + pr * 4;
    f4 kt[4];
    const float k2048 = kgen_chain(sm, kc + hr * KT_BYTES, l, R, kt, [] {});
    a_finish<true>(P, sm, sb, R, kt, k2048, ch, hr, w, l, tag, kc + hr * KT_BYTES, kc + (hr ^ 1) * KT_BYTES, S_MISC + 48 + pr * 8, S_XY,
                   S_KD + pr * KD_BYTES, kc, S_UB + pr * UB_BYTES, cnt, rs, b,
                   [&] {
                     if (w == 0) {
                       lds_wait(sm, S_MISC + 44);
                       __builtin_amdgcn_s_sleep(10);
                       wait_lane_flags<16>(ws, WS_UFLAG + l * 128, tag, l, 1);
                       lds_set(sm, S_MISC + 40, l);
                     } else lds_wait(sm, S_MISC + 40);
                     fetch_u(ws, sb, ch, hr, l, S_UB + pr * UB_BYTES);
                     lds_wait(sm, S_MISC + 36);
                   }, [] { vm0(); } STV_ARG);
  } else {
    __builtin_amdgcn_s_setprio(3);
    const u32 cnt = S_MISC + 16 + 12;
    const v4u gw0 = l < 36 ? *(const GAS v4u*)((const char*)P.gw + (hr * 36 + l) * 16) : (v4u){0, 0, 0, 0};
    if (l < 36) stl128(sm, S_GW + (hr * 36 + l) * 16, gw0);
    vm0();
    PIN(ev);
    const u64 tag = (((u64)ev[1] << 32) | ev[0]) + 1;
    arrive(sm, S_MISC + 32, l);
    pair_sync(sm, cnt, 2, l);
    stamp(ws, b, w, l, 1);
    {
      const int g = l >> 4, q = (l & 15) >> 2, p = l & 3;
      const u32 st = S_ST + hr * 9216;
#pragma unroll 1
      for (int r = 0; r < 2; ++r) {
#pragma unroll
        for (int i = 0; i < 9; ++i) {
          const u32 a0 = S_X + (8 * g + q) * 1152 + ((18 * hr + 9 * r + i) * 16 + 4 * p) * 2;
          const v2u lo = ldstr(sm, a0), hi = ldstr(sm, a0 + 4 * 1152);
          stl128(sm, st + i * 1024 + (l & 15) * 64 + g * 16, (v4u){lo[0], lo[1], hi[0], hi[1]});
        }
#pragma unroll
        for (int i = 0; i < 9; ++i)
          __builtin_amdgcn_raw_buffer_store_b128(lds128(sm, st + i * 1024 + l * 16), rs,
                                                 WS_UT + (u32)((18 * hr + 9 * r + i) * 16 + (l >> 2)) * 4096u + sidx * 64 + (l & 3) * 16, 0, 16);
      }
    }
    vm0();
    pair_sync(sm, cnt, 4, l);
    if (hr == 0 && l == 0) { stg64(ws + WS_UFLAG + sidx * 128, tag); lds_set(sm, S_MISC + 44, l); }
    stamp(ws, b, w, l, 2);
#pragma unroll 1
    for (int fl = hr; fl < 21; fl += 2) {
      const int f = sidx * 21 + fl;
      if (f < 1296)
        __builtin_amdgcn_raw_buffer_store_b128(lds128(sm, S_XY + fl * 1024 + (l & 15) * 64 + 16 * ((l >> 4) ^ (((l & 15) >> 1) & 3))), rs,
                                               WS_CT + (u32)f * 1024u + l * 16, 0, 16);
    }
    vm0();
    pair_sync(sm, cnt, 6, l);
    if (hr == 0 && l == 0) { stg64(ws + WS_CFLAG + sidx * 128, tag); lds_set(sm, S_MISC + 36, l); }
    stamp(ws, b, w, l, 3);
#pragma unroll 1
    for (int r = 0; r < 2; ++r) {
      const int row = 16 * hr + 8 * r + (l >> 3), q = l & 7;
      float s = 0.f;
#pragma unroll 3
      for (int i = 0; i < 9; ++i) {
        const v4u xv = lds128(sm, S_X + row * 1152 + (q + 8 * i) * 16), wv = lds128(sm, S_GW + (q + 8 * i) * 16);
#pragma unroll
        for (int k = 0; k < 4; ++k) {
          const u32 pp = pk2(bflo(xv[k]) * bflo(wv[k]), bfhi(xv[k]) * bfhi(wv[k]));
          s += bflo(pp) + bfhi(pp);
        }
      }
      s += __shfl_xor(s, 1); s += __shfl_xor(s, 2); s += __shfl_xor(s, 4);
      const float gv = rbf(1.f / (1.f + exp_acc(-rbf(s))));
      if (q == 0) stgf(ws + WS_G + (sidx * 32 + row) * 4, gv);
    }
    vm0();
    pair_sync(sm, cnt, 8, l);
    if (hr == 0) {
      lds_wait_count(sm, S_MISC + 32, 8);
      if (l == 0) { stg64(ws + WS_GFLAG + sidx * 128, tag); stg64(ws + WS_TOK + b * 8, tag); }
    }
    stamp(ws, b, w, l, 5);
    wait_lane_flags<32>(ws, WS_CFLAG + (l < 62 ? l : 0) * 128, tag, l, 10);
    {
      const char* cb = ws + WS_CT + (size_t)(((sidx >> 3) & 1) * 648 + (sidx & 7) * 81 + hr * 40) * 1024;
#pragma unroll 1
      for (int i = 0; i < 7; ++i) dma6_lin((u32)(l * 16), cb + (i < 6 ? i * 6144 : 35 * 1024), sb + S_ST + hr * 9216);
      vm0();
    }
    if (sidx == 63 && hr == 1) {
#pragma unroll 1
      for (int i = 0; i < 4; ++i) wait_lane_flags<8>(ws, WS_TOK + i * 512 + l * 8, tag, l, 9);
      if (l == 0) stg64(ws + WS_E, tag);
    }
    stamp(ws, b, w, l, 4);
  }
}

struct LC { float a, s; };
DEVI LC lamc(LAS char* sm, u32 lt, int pl, int k, int comp) {
  const v2u v = lds64(sm, lt + pl * 72 + k * 8);
  const float zi = __uint_as_float(v[1]);
  return LC{__uint_as_float(v[0]), comp ? zi : -zi};
}

DEVI void b_wg(const Params& P, LAS char* sm, int c, int j, int b, int w, int l, int tid STV_PARAM) {
  char* ws = P.ws;
  const u32 sb = (u32)(size_t)sm;
  const __amdgpu_buffer_rsrc_t rs = __builtin_amdgcn_make_buffer_rsrc(ws, 0, WS_BYTES, 0x00020000);
  const int bw = c * 6 + j;
  const int x = l & 15, G = l >> 4;
  const int dh = w & 1, kq = w >> 1;
  u64 tag;
  v4u Cf[9][3];
  v2u xv[3], dv[3];
  auto load_xd = [&]() {
#pragma unroll
    for (int dt = 0; dt < 3; ++dt) {
      const u32 dg = 96 * j + 48 * dh + 16 * dt + 4 * G;
      xv[dt] = ald_64((u32)(c * 64 + 16 * kq + x) * 1152 + dg * 2, P.u);
      dv[dt] = ald_64(dg * 2, P.Dsk2);
    }
  };
  auto load_c = [&]() {
    const char* cb = ws + WS_CT + (size_t)(((j * 2 + dh) * 4 + kq) * 27) * 1024;
#pragma unroll
    for (int i = 0; i < 7; ++i) {
      const u32 o = (u32)(l * 16 + i * 4096);
      Cf[(4 * i) / 3][(4 * i) % 3] = ald_128_sc1o<0>(o, cb);
      Cf[(4 * i + 1) / 3][(4 * i + 1) % 3] = ald_128_sc1o<1024>(o, cb);
      Cf[(4 * i + 2) / 3][(4 * i + 2) % 3] = ald_128_sc1o<2048>(o, cb);
      if (i < 6) Cf[(4 * i + 3) / 3][(4 * i + 3) % 3] = ald_128_sc1o<3072>(o, cb);
    }
    load_xd();
  };
  auto pin_c = [&]() {
#pragma unroll
    for (int i = 0; i < 9; ++i)
#pragma unroll
      for (int dt = 0; dt < 3; ++dt) PIN(Cf[i][dt]);
#pragma unroll
    for (int i = 0; i < 3; ++i) { PIN(xv[i]); PIN(dv[i]); }
  };
  if (w < 4) {
    const int pr = w >> 1, hr = w & 1, ch = 2 * bw + pr;
    ARaw R;
    a_load_modes(P, ch, hr, l, R); a_load_tab(P, ch, hr, l, R);
    v2u ev = ald_64_sc1(0, ws + WS_E);
    const int rot = ((c & 7) + 3 * (j % 3)) % 9;
    const int rr = l >> 3;
    u32 vx[2], vb[6];
#pragma unroll
    for (int i = 0; i < 2; ++i) {
      const int t = 16 * w + 8 * i + rr, f = 2 * (t >> 4) + ((t >> 1) & 1);
      vx[i] = (u32)((c * 64 + t) * 1152 + 16 * ((l & 7) ^ f) + 4096 - 1024 * i);
    }
#pragma unroll
    for (int k = 0; k < 6; ++k)
      vb[k] = (u32)((96 * j + 8 * (3 * w + (k >> 1)) + rr) * 1152 + 16 * ((l & 7) ^ (4 * (k & 1) + (rr >> 1))) + 4096 - 1024 * (k & 3));
    const char* xb0 = (const char*)P.u - 4096;
    const char* br0 = (const char*)P.Bre - 4096;
    const char* bi0 = (const char*)P.Bim - 4096;
    auto issue = [&](int s, u32 so) {
      int ks = s + rot; if (ks >= 9) ks -= 9;
      const u32 slot = sb + B_RING + so;
      dma2(vx[0], vx[1], xb0 + ks * 128, slot + 2 * w * 1024);
      dma6(vb[0], vb[1], vb[2], vb[3], vb[4], vb[5], br0 + ks * 128, bi0 + ks * 128, slot + 8192 + 6 * w * 1024);
    };
    issue(0, 0); issue(1, 32768);
    a_zero_pad(sm, B_UB + pr * UB_BYTES, hr, l);
    if (tid < 8) *(LAS u32*)(sm + B_MISC + 16 + tid * 4) = 0;
    vmw<16>();
    a_pin_modes(R); a_pin_tab(R); PIN(ev);
    tag = (((u64)ev[1] << 32) | ev[0]) + 1;
    if (tid == 0) *(LAS u64*)(sm + B_MISC) = tag;
    stamp(ws, b, w, l, 1);
    u32 nxt = 65536;
#pragma unroll 1
    for (int s = 0; s < 9; ++s) {
      if (s < 8) vmw<8>(); else vm0();
      barrier();
      if (s < 7) { issue(s + 2, nxt); nxt = nxt == 65536 ? 0 : nxt + 32768; }
    }
    stamp(ws, b, w, l, 2);
    v2u uf;
    if (tid == 0) stg64(ws + WS_TOK + b * 8, tag);
    f4 kt[4];
    const float k2048 = kgen_chain(sm, B_KT + w * KT_BYTES, l, R, kt, [&] { if (w == 0) uf = ald_64_sc1o<0>(WS_UFLAG + l * 128, ws); });
    stamp(ws, b, w, l, 14);
    if (w == 0) {
      vm0(); PIN(uf);
      if (!__all(uf[0] == (u32)tag && uf[1] == (u32)(tag >> 32))) wait_lane_flags<6>(ws, WS_UFLAG + l * 128, tag, l, 1);
      lds_set(sm, B_MISC + 40, l);
    } else lds_wait(sm, B_MISC + 40);
    fetch_u(ws, sb, ch, hr, l, B_UB + pr * UB_BYTES);
    a_finish<false>(P, sm, sb, R, kt, k2048, ch, hr, w, l, tag, B_XK + w * 4096, B_XK + (w ^ 1) * 4096, B_MISC + 48 + pr * 8, B_XY, B_KD + pr * KD_BYTES,
                    B_KC + pr * KC_BYTES, B_UB + pr * UB_BYTES, B_MISC + 16 + pr * 4, rs, b, [] {},
                    [&] {
                      lds_wait(sm, B_MISC + 44);
                      load_c();
                      vm0();
                    } STV_ARG);
    pin_c();
  } else {
    __builtin_amdgcn_s_setprio(2);
    const int cg = w - 4, comp = x >> 3;
    const u32 lt = B_LT + cg * LT_BYTES;
    if (l < 24) {
      const int p = 96 * j + 24 * cg + l;
      const float lre = bf2f(ldu16(P.Lre + p)), lim = bf2f(ldu16(P.Lim + p));
      const float dt = rbf(exp_acc(bf2f(ldu16(P.logdt2 + p))));
      const float e = exp_acc(dt * lre);
      const f2 cs = cis(dt * lim);
      const f2 lam = {e * cs.x, e * cs.y};
      const float nr = lam.x - 1.f, ni = lam.y, den = lre * lre + lim * lim;
      const f2 coef = {(nr * lre + ni * lim) / den, (ni * lre - nr * lim) / den};
      const f2 z16 = csq(csq(csq(csq(lam)))), z32 = csq(z16), z48 = cmul(z32, z16), mu = csq(z32);
      const f2 m8 = csq(csq(csq(mu))), m16 = csq(m8), m24 = cmul(m16, m8);
      LAS f2* t = (LAS f2*)(sm + lt + l * 72);
      t[0] = lam; t[1] = z16; t[2] = z32; t[3] = z48; t[4] = mu; t[5] = m8; t[6] = m16; t[7] = m24; t[8] = coef;
    }
    stamp(ws, b, w, l, 1);
    f4 g1[4][3] = {};
    {
      const int t0 = 16 * (x >> 2) + (x & 3), f = 2 * (x >> 2) + ((x >> 1) & 1);
      const u32 aa0 = t0 * 128 + 16 * (G ^ f), aa1 = aa0 ^ 64;
      const u32 bb0 = 8192 + (48 * cg + x) * 128 + 16 * (G ^ (x >> 1)), bb1 = bb0 ^ 64;
      u32 so = B_RING;
      v4u A0[4], B0[3], A1[4], B1[3];
      auto ldf = [&](v4u (&A)[4], v4u (&B)[3], u32 aa, u32 bb) {
        B[0] = lds128(sm, so + bb); A[0] = lds128(sm, so + aa); B[1] = lds128(sm, so + bb + 2048); B[2] = lds128(sm, so + bb + 4096);
#pragma unroll
        for (int mt = 1; mt < 4; ++mt) A[mt] = lds128(sm, so + aa + 512 * mt);
      };
      auto mm = [&](const v4u (&A)[4], const v4u (&B)[3]) {
#pragma unroll
        for (int mt = 0; mt < 4; ++mt)
#pragma unroll
          for (int nt = 0; nt < 3; ++nt) g1[mt][nt] = mfma16(A[mt], B[nt], g1[mt][nt]);
      };
      auto mix = [&]() {
#pragma unroll
        for (int i = 0; i < 5; ++i) { __builtin_amdgcn_sched_group_barrier(0x008, 2, 0); __builtin_amdgcn_sched_group_barrier(0x100, 1, 0); }
        __builtin_amdgcn_sched_group_barrier(0x008, 1, 0); __builtin_amdgcn_sched_group_barrier(0x100, 1, 0);
        __builtin_amdgcn_sched_group_barrier(0x008, 1, 0); __builtin_amdgcn_sched_group_barrier(0x100, 1, 0);
      };
      barrier();
      ldf(A0, B0, aa0, bb0); ldf(A1, B1, aa1, bb1);
      SB(); mm(A0, B0); SB();
#pragma unroll 1
      for (int s = 1; s < 9; ++s) {
        so = so == 65536 ? 0 : so + 32768;
        barrier();
        SB();
        ldf(A0, B0, aa0, bb0); mm(A1, B1); mix();
        SB();
        ldf(A1, B1, aa1, bb1); mm(A0, B0); mix();
        SB();
      }
      mm(A1, B1);
    }
    tag = *(LAS u64*)(sm + B_MISC);
    v2u cf;
    if (w == 4) cf = ald_64_sc1o<0>(WS_CFLAG + (l < 62 ? l : 0) * 128, ws);
    stamp(ws, b, w, l, 3);
    const u32 taglo = (u32)tag, taghi = (u32)(tag >> 32) & 0xffu;
    float X[3], la[3], ls[3];
    {
      float h[3];
#pragma unroll
      for (int nt = 0; nt < 3; ++nt) { const LC q = lamc(sm, lt, 8 * nt + (x & 7), 0, comp); la[nt] = q.a; ls[nt] = q.s; h[nt] = g1[0][nt][0]; }
#pragma unroll
      for (int tau = 1; tau < 16; ++tau) scan_keep(h[0], h[1], h[2], g1[tau >> 2][0][tau & 3], g1[tau >> 2][1][tau & 3], g1[tau >> 2][2][tau & 3], la, ls);
      float tot[3];
#pragma unroll
      for (int nt = 0; nt < 3; ++nt) {
        const LC q = lamc(sm, lt, 8 * nt + (x & 7), G == 3 ? 1 : 3 - G, comp);
        const float sc = lmul_add(q.a, q.s, h[nt], 0.f);
        float a = G == 3 ? h[nt] : sc;
        auto r1 = __builtin_amdgcn_permlane16_swap(__float_as_uint(a), __float_as_uint(a), false, false);
        a = __uint_as_float(r1[0]) + __uint_as_float(r1[1]);
        auto r2 = __builtin_amdgcn_permlane32_swap(__float_as_uint(a), __float_as_uint(a), false, false);
        tot[nt] = __uint_as_float(r2[0]) + __uint_as_float(r2[1]);
      }
      if (G < 2) {
        const u32 o = G == 0 ? WS_S + (u32)((c * 72 + 12 * j + 3 * cg) * 128) : WS_Z + (u32)(bw * 4 + cg) * 384u;
#pragma unroll
        for (int nt = 0; nt < 3; ++nt) {
          const u32 hi = (G == 0 ? (__float_as_uint(tot[nt]) + 0x80u) & 0xffffff00u : 0u) | taghi;
          stg64(ws + o + nt * 128 + x * 8, ((u64)hi << 32) | taglo);
        }
      }
#pragma unroll
      for (int nt = 0; nt < 3; ++nt) {
        const int pl = 8 * nt + (x & 7);
        const LC l16 = lamc(sm, lt, pl, 1, comp), l32 = lamc(sm, lt, pl, 2, comp);
        float e = h[nt];
        const float v1 = __uint_as_float(__builtin_amdgcn_permlane16_swap(__float_as_uint(e), __float_as_uint(e), false, false)[0]);
        const bool odd = G & 1;
        e = lmul_add(odd ? l16.a : 0.f, odd ? l16.s : 0.f, v1, e);
        const u32 r1 = __builtin_amdgcn_permlane16_swap(__float_as_uint(e), __float_as_uint(e), false, false)[1];
        const float v2 = __uint_as_float(__builtin_amdgcn_permlane32_swap(r1, r1, false, false)[0]);
        e = lmul_add(G == 2 ? l16.a : 0.f, G == 2 ? l16.s : 0.f, v2, e);
        const float v3 = __uint_as_float(__builtin_amdgcn_permlane16_swap(__float_as_uint(e), __float_as_uint(e), false, false)[0]);
        X[nt] = G == 0 ? 0.f : G == 2 ? v2 : v3;
      }
    }
    __builtin_amdgcn_s_setprio(0);
    stamp(ws, b, w, l, 4);
    float val[3][8];
    bool c_early;
    {
      const u32 zoff = WS_Z + (u32)(bw * 4 + cg) * 384u + x * 8;
      u32 ad[8];
#pragma unroll
      for (int i = 0; i < 8; ++i) { const int cp = c - 32 + 8 * G + i; ad[i] = cp >= 0 ? WS_S + (u32)((cp * 72 + 12 * j + 3 * cg) * 128 + x * 8) : zoff; }
      if (w == 4) { vm0(); PIN(cf); c_early = __all(cf[0] == (u32)tag && cf[1] == (u32)(tag >> 32)); lds_set(sm, c_early ? B_MISC + 44 : B_MISC + 24, l); }
      else {
        for (u32 s = 0; s < (1u << 22); ++s) {
          const v2u v = lds64(sm, B_MISC + 24), v2 = lds64(sm, B_MISC + 40);
          asm volatile("" ::: "memory");
          c_early = rfl((int)v2[1]) != 0;
          if (c_early || rfl((int)v[0]) != 0) break;
          __builtin_amdgcn_s_sleep(1);
        }
      }
      if (c_early) load_c(); else __builtin_amdgcn_s_sleep(12);
      stamp(ws, b, w, l, 13);
      const int cpl = c - 32 + (l & 31);
      const u32 pa = cpl >= 0 ? WS_S + (u32)((cpl * 72 + 12 * j + 3 * cg + 2) * 128 + 120) : zoff;
      for (u32 sp = 0;; ++sp) {
        v2u gr[8][3];
#pragma unroll
        for (int i = 0; i < 8; ++i) { gr[i][0] = ald_64_sc1o<0>(ad[i], ws); gr[i][1] = ald_64_sc1o<128>(ad[i], ws); gr[i][2] = ald_64_sc1o<256>(ad[i], ws); }
        vm0();
        u32 bad = 0;
#pragma unroll
        for (int i = 0; i < 8; ++i)
#pragma unroll
          for (int nt = 0; nt < 3; ++nt) {
            PIN(gr[i][nt]);
            bad |= (gr[i][nt][0] ^ taglo) | ((gr[i][nt][1] ^ taghi) & 0xffu);
            val[nt][i] = __uint_as_float(gr[i][nt][1] & 0xffffff00u);
          }
        if (__all(bad == 0)) break;
        bool out = false;
        for (;; ++sp) {
          __builtin_amdgcn_s_sleep(3);
          v2u pv = ald_64_sc1o<0>(pa, ws);
          vm0(); PIN(pv);
          if (__all(pv[0] == taglo && (pv[1] & 0xffu) == taghi)) break;
          if (sp > SPIN_MAX) { if (l == 0) spin_fail(ws, 2); out = true; break; }
        }
        if (out) break;
      }
    }
    stamp(ws, b, w, l, 5);
#pragma unroll
    for (int nt = 0; nt < 3; ++nt) {
      const int pl = 8 * nt + (x & 7);
      const LC mu = lamc(sm, lt, pl, 4, comp);
      float a = val[nt][0];
#pragma unroll
      for (int i = 1; i < 8; ++i) a = lmul_add(mu.a, mu.s, a, val[nt][i]);
      const LC mg = lamc(sm, lt, pl, G == 3 ? 5 : 7 - G, comp);
      const float sc = lmul_add(mg.a, mg.s, a, 0.f);
      a = G == 3 ? a : sc;
      auto r1 = __builtin_amdgcn_permlane16_swap(__float_as_uint(a), __float_as_uint(a), false, false);
      a = __uint_as_float(r1[0]) + __uint_as_float(r1[1]);
      auto r2 = __builtin_amdgcn_permlane32_swap(__float_as_uint(a), __float_as_uint(a), false, false);
      const float carry = __uint_as_float(r2[0]) + __uint_as_float(r2[1]);
      const LC lg = lamc(sm, lt, pl, G == 0 ? 1 : G, comp);
      const float s2 = lmul_add(lg.a, lg.s, carry, X[nt]);
      X[nt] = G == 0 ? carry : s2;
    }
    {
      float ca[3], cs[3];
      u32 base[3];
#pragma unroll
      for (int nt = 0; nt < 3; ++nt) {
        const LC cf = lamc(sm, lt, 8 * nt + (x & 7), 8, comp);
        ca[nt] = comp ? -cf.a : cf.a; cs[nt] = comp ? -cf.s : cf.s;
        const int pg = 12 * j + 3 * cg + nt, jj = x & 7;
        base[nt] = WS_HT + (u32)((c * 72 + pg) * 2 + (G >> 1)) * 1024u +
                   ((jj & 3) | (((pg & 1) ^ comp) << 3) | ((jj >> 2) << 4) | ((G & 1) << 5)) * 16;
      }
#pragma unroll
      for (int u = 0; u < 2; ++u) {
        float o[3][8];
#pragma unroll
        for (int tau = 0; tau < 8; ++tau) {
          const int tt = 8 * u + tau;
          scan_out(X[0], X[1], X[2], g1[tt >> 2][0][tt & 3], g1[tt >> 2][1][tt & 3], g1[tt >> 2][2][tt & 3], la, ls, ca, cs, o[0][tau], o[1][tau], o[2][tau]);
        }
#pragma unroll
        for (int nt = 0; nt < 3; ++nt) {
          const v4u v = {pk2(o[nt][0], o[nt][1]), pk2(o[nt][2], o[nt][3]), pk2(o[nt][4], o[nt][5]), pk2(o[nt][6], o[nt][7])};
          __builtin_amdgcn_raw_buffer_store_b128(v, rs, base[nt] + u * 64, 0, 16);
        }
      }
    }
    vm0();
    if (l == 0) stg64(ws + WS_HFLAG + bw * 128 + cg * 8, tag);
    stamp(ws, b, w, l, 6);
    if (!c_early) {
      if (w == 4) { wait_lane_flags<16>(ws, WS_CFLAG + (l < 62 ? l : 0) * 128, tag, l, 4); lds_set(sm, B_MISC + 44, l); } else lds_wait(sm, B_MISC + 44);
      load_c();
      vm0();
    }
    pin_c();
  }
  float gv[4];
  stamp(ws, b, w, l, 8);
  if (w == 0) wait_lane_flags<2>(ws, WS_HFLAG + (c * 6 + (l < 24 ? l >> 2 : 0)) * 128 + (l & 3) * 8, tag, l, 5);
  barrier();
  stamp(ws, b, w, l, 9);
  auto stage_h = [&](int be) {
    const int q = w & 3;
#pragma unroll
    for (int half = 0; half < 2; ++half)
      dma6_lin((u32)(l * 16), ws + WS_HT + (size_t)((c * 72 + 12 * (be + 3 * half)) * 2 + q * 6) * 1024, sb + be * 49152 + (half * 24 + q * 6) * 1024);
  };
  stage_h(w >> 2);
  f4 o2[3][4] = {};
  {
    const int cmp = kq >> 1, hf = kq & 1;
    const int q = (l & 15) >> 2, p = l & 3;
    u32 ta2[2];
#pragma unroll
    for (int t = 0; t < 2; ++t) {
      const int jj = 4 * t + q;
      const int pos = (jj & 3) | (((p >> 1) & 1) << 2) | (((G & 1) ^ cmp) << 3) | ((jj >> 2) << 4);
      ta2[t] = (hf * 24 + G * 2) * 1024 + pos * 16 + 8 * (p & 1);
    }
    PIN(ta2[1]);
    v4u Bf[2][4];
    auto ldB = [&](int kk, v4u (&Bv)[4]) {
#pragma unroll
      for (int nu = 0; nu < 4; ++nu) {
        const u32 o = (kk / 3) * 49152 + (kk % 3) * 8192 + (nu >> 1) * 1024 + (nu & 1) * 512;
        const v2u lo = ldstr(sm, ta2[0] + o), hi = ldstr(sm, ta2[1] + o);
        Bv[nu] = (v4u){lo[0], lo[1], hi[0], hi[1]};
      }
    };
    stamp(ws, b, w, l, 17);
    if (w < 4) vm0();
    barrier();
    stamp(ws, b, w, l, 18);
    if (w < 4) stage_h(2);
    ldB(0, Bf[0]);
#pragma unroll
    for (int kk = 0; kk < 9; ++kk) {
      if (kk == 2) { stamp(ws, b, w, l, 19); if (w >= 4) vm0(); barrier(); stamp(ws, b, w, l, 20); }
      if (kk == 5) { if (w == 3) wait_lane_flags<4>(ws, WS_GFLAG + (2 * c + (l & 1)) * 128, tag, l, 8); stamp(ws, b, w, l, 21); vm0(); barrier(); stamp(ws, b, w, l, 22); }
      if (kk == 5) {
        if (w < 3) {
          wait_lane_flags<2>(ws, WS_AFLAG + (96 * j + 32 * w + (l >> 1)) * 128 + (l & 1) * 8, tag, l, 6);
#pragma unroll
          for (int i = 0; i < 4; ++i)
            dma_sc1((u32)((96 * j + 32 * w + 8 * i + (l >> 3)) * 4096 + c * 128 + (l & 7) * 16), ws + WS_AT, sb + B_AT + (4 * w + i) * 1024);
        }
#pragma unroll
        for (int nu = 0; nu < 4; ++nu) gv[nu] = __uint_as_float(ald_32_sc1((u32)(WS_G + (c * 64 + 16 * nu + x) * 4), ws));
      }
      SB();
      if (kk < 8) ldB(kk + 1, Bf[(kk + 1) & 1]);
#pragma unroll
      for (int nu = 0; nu < 4; ++nu)
#pragma unroll
        for (int dt = 0; dt < 3; ++dt) o2[dt][nu] = mfma16(Cf[kk][dt], Bf[kk & 1][nu], o2[dt][nu]);
      if (kk < 8) {
#pragma unroll
        for (int i = 0; i < 4; ++i) { __builtin_amdgcn_sched_group_barrier(0x008, 1, 0); __builtin_amdgcn_sched_group_barrier(0x100, 1, 0);
                                      __builtin_amdgcn_sched_group_barrier(0x008, 2, 0); __builtin_amdgcn_sched_group_barrier(0x100, 1, 0); }
      }
      SB();
    }
  }
  stamp(ws, b, w, l, 10);
  vm0();
#pragma unroll
  for (int i = 0; i < 4; ++i) PIN(gv[i]);
  stamp(ws, b, w, l, 23);
  barrier();
  stamp(ws, b, w, l, 24);
#pragma unroll
  for (int nu = 0; nu < 4; ++nu) {
    const float om = rbf(1.f - gv[nu]);
#pragma unroll
    for (int dt = 0; dt < 3; ++dt) {
      f4 v = o2[dt][nu] * (2.f * om);
      if (nu == kq) {
        const int dl = 48 * dh + 16 * dt + 4 * G;
        const v2u av = ldstr(sm, B_AT + (dl + ((l & 15) >> 2)) * 128 + (16 * nu + 4 * (l & 3)) * 2);
        v[0] += rbf(gv[nu] * bflo(av[0])) + om * rbf(bflo(xv[dt][0]) * bflo(dv[dt][0]));
        v[1] += rbf(gv[nu] * bfhi(av[0])) + om * rbf(bfhi(xv[dt][0]) * bfhi(dv[dt][0]));
        v[2] += rbf(gv[nu] * bflo(av[1])) + om * rbf(bflo(xv[dt][1]) * bflo(dv[dt][1]));
        v[3] += rbf(gv[nu] * bfhi(av[1])) + om * rbf(bfhi(xv[dt][1]) * bfhi(dv[dt][1]));
      }
      stl128(sm, B_RED + kq * RED_Q + (16 * nu + x) * RED_ROW + (48 * dh + 16 * dt + 4 * G) * 4, __builtin_bit_cast(v4u, v));
    }
  }
  stamp(ws, b, w, l, 25);
  barrier();
  stamp(ws, b, w, l, 26);
#pragma unroll
  for (int i = 0; i < 3; ++i) {
    const int e = tid + 512 * i, t = e / 24, d4 = e % 24;
    const u32 o = B_RED + t * RED_ROW + d4 * 16;
    const f4 v = __builtin_bit_cast(f4, lds128(sm, o)) + __builtin_bit_cast(f4, lds128(sm, o + RED_Q)) +
                 __builtin_bit_cast(f4, lds128(sm, o + 2 * RED_Q)) + __builtin_bit_cast(f4, lds128(sm, o + 3 * RED_Q));
    *(GAS f4*)(P.out + (size_t)(c * 64 + t) * D_ + j * 96 + d4 * 4) = v;
  }
  stamp(ws, b, w, l, 11);
}

__global__ void __launch_bounds__(512, 2) gated_mix_kernel(Params P) {
  extern __shared__ __attribute__((aligned(16))) char smem_raw[];
  LAS char* sm = (LAS char*)smem_raw;
  const int tid = threadIdx.x, l = tid & 63, w = rfl(tid >> 6);
  const int b = blockIdx.x, xcd = b & 7, r = b >> 3;
  STAMP_DECL;
  stamp(P.ws, b, w, l, 0);
  stampc(P.ws, b, w, l, 30);
  {
    const u64 pc = __builtin_amdgcn_s_getpc() & ~(u64)255;
    const int piece = r * 8 + w;
    if (piece < CODE_KB) dma((u32)(piece * 1024 + l * 16), (const void*)pc, (u32)(size_t)sm + LDS_BYTES - 1024);
  }
  if (r < 24) b_wg(P, sm, (xcd >> 1) * 8 + r / 3, (xcd & 1) * 3 + r % 3, b, w, l, tid STV_ARG);
  else spare_wg(P, sm, xcd * 8 + (r - 24), b, w, l, tid STV_ARG);
  stamp(P.ws, b, w, l, 12);
  stampc(P.ws, b, w, l, 31);
  STAMP_FLUSH(P.ws, b, w, l);
}

extern "C" void kernel_launch(void* const* d_in, const int* in_sizes, int n_in, void* d_out, int out_size, void* d_ws, size_t ws_size,
                              hipStream_t stream) {
  Params P;
  P.u = (const u16*)d_in[0]; P.logA = (const u16*)d_in[1]; P.Aim = (const u16*)d_in[2]; P.Cre = (const u16*)d_in[3]; P.Cim = (const u16*)d_in[4];
  P.logdt = (const u16*)d_in[5]; P.Dsk = (const u16*)d_in[6];
  P.Lre = (const u16*)d_in[8]; P.Lim = (const u16*)d_in[9]; P.Bre = (const u16*)d_in[10]; P.Bim = (const u16*)d_in[11];
  P.C2re = (const u16*)d_in[12]; P.C2im = (const u16*)d_in[13]; P.logdt2 = (const u16*)d_in[14]; P.Dsk2 = (const u16*)d_in[15]; P.gw = (const u16*)d_in[16];
  P.out = (float*)d_out; P.ws = (char*)d_ws;
  (void)hipFuncSetAttribute((const void*)gated_mix_kernel, hipFuncAttributeMaxDynamicSharedMemorySize, LDS_BYTES);
  hipLaunchKernelGGL(gated_mix_kernel, dim3(256), dim3(512), LDS_BYTES, stream, P);
}
```
